```python
import torch

def _softmax(x, axis=-1):
    return torch.softmax(x, dim=axis)
def _gelu(x):
    return torch.nn.functional.gelu(x, approximate="tanh")
def _silu(x):
    return torch.nn.functional.silu(x)
def _sigmoid(x):
    return torch.sigmoid(x)
def _layernorm(x, axis=-1, eps=1e-5):
    m = x.mean(dim=axis, keepdim=True)
    v = x.var(dim=axis, keepdim=True, unbiased=False)
    return (x - m) / torch.sqrt(v + eps)
def _scatter_add(tgt, idx, src, axis=0):
    return tgt.clone().index_add_(axis, idx, src)
def _segment_sum(src, seg, n):
    out = torch.zeros((n,) + src.shape[1:], dtype=src.dtype)
    return out.index_add_(0, seg, src)
def _pad(x, pw):
    flat = tuple(v for pair in reversed(pw) for v in pair)
    return torch.nn.functional.pad(x, flat)

kernel_name = 'gated_mix__ssm_conv_s4__linear_attn_block__prompt'

def setup_inputs():

    _w = (lambda a: (lambda t: t.to(torch.bfloat16) if t.is_floating_point() else t)(torch.as_tensor(a)))
    def _A_setup():
        B, L, D, N = 1, 2048, 640, 128
        A_re = -(lambda _x: torch.exp(torch.as_tensor(_x)))((lambda *s: torch.randn(*s, dtype=torch.bfloat16) * 3.2)(D, N))
        A_im = torch.pi * (lambda *s: torch.randn(*s, dtype=torch.bfloat16) * 0.05)(D, N)
        B_re = (lambda *s: torch.randn(*s, dtype=torch.bfloat16) * 0.2)(D, N)
        B_im = (lambda *s: torch.randn(*s, dtype=torch.bfloat16) * 0.2)(D, N)
        C_re = (lambda *s: torch.randn(*s, dtype=torch.bfloat16) * 0.2)(D, N)
        C_im = (lambda *s: torch.randn(*s, dtype=torch.bfloat16) * 0.2)(D, N)
        log_dt = (lambda *s: torch.randn(*s, dtype=torch.bfloat16) * 0.05)(D) * 0.5 - 4.0
        Dskip = (lambda *s: torch.randn(*s, dtype=torch.bfloat16) * 0.05)(D)
        return {
            "u": _w((lambda *s: torch.randn(*s, dtype=torch.bfloat16) * 0.05)(B, L, D)),
            "A_re": _w(A_re),
            "A_im": _w(A_im),
            "B_re": _w(B_re),
            "B_im": _w(B_im),
            "C_re": _w(C_re),
            "C_im": _w(C_im),
            "log_dt": _w(log_dt),
            "Dskip": _w(Dskip),
        }
    _A_in = _A_setup()

    def _B_setup():
        B, S, D = 1, 2048, 640
        return {
            "x": _w((lambda *s: torch.randn(*s, dtype=torch.bfloat16) * 0.05)(B, S, D)),
            "wqkv": _w((lambda *s: torch.randn(*s, dtype=torch.bfloat16) * 0.8)(D, 3 * D)),
            "wo": _w((lambda *s: torch.randn(*s, dtype=torch.bfloat16) * 0.05)(D, D)),
        }
    _B_in = _B_setup()

    return {
        "A_u": _A_in["u"],
        "A_A_re": _A_in["A_re"],
        "A_A_im": _A_in["A_im"],
        "A_B_re": _A_in["B_re"],
        "A_B_im": _A_in["B_im"],
        "A_C_re": _A_in["C_re"],
        "A_C_im": _A_in["C_im"],
        "A_log_dt": _A_in["log_dt"],
        "A_Dskip": _A_in["Dskip"],
        "B_x": _B_in["x"],
        "B_wqkv": _B_in["wqkv"],
        "B_wo": _B_in["wo"],
        "gate_w": _w((lambda *s: torch.randn(*s, dtype=torch.bfloat16) * 0.05)(640) * 16.0),
    }

def reference(A_u, A_A_re, A_A_im, A_B_re, A_B_im, A_C_re, A_C_im, A_log_dt, A_Dskip, B_x, B_wqkv, B_wo, gate_w):
    def _A_ref(u, A_re, A_im, B_re, B_im, C_re, C_im, log_dt, Dskip):
        Bsz, L, D = u.shape
        dt = torch.exp(log_dt)[:, None]
        A = A_re + 1j * A_im
        Bc = B_re + 1j * B_im
        Cc = C_re + 1j * C_im
        logdA = dt * A
        dB = dt * Bc
        l = (lambda x, dt: x.to(dt))(torch.arange(L), A_re.dtype)
        P = torch.exp(logdA[:, :, None] * l[None, None, :])
        K = torch.einsum("dn,dnl,dn->dl", Cc, P, dB).real
        L2 = 2 * L
        Kp = _pad(K, ((0, 0), (0, L)))
        up = (lambda x, *d: x.permute(*d))(u, 0, 2, 1)
        up = _pad(up, ((0, 0), (0, 0), (0, L)))
        Kf = (lambda x, axes=(-2,-1): torch.fft.rfft2(x.float(), dim=axes))(Kp[:, None, :], axes=(-2, -1))
        Uf = (lambda x, axes=(-2,-1): torch.fft.rfft2(x.float(), dim=axes))(up[:, :, None, :], axes=(-2, -1))
        Yf = Uf * Kf[None, :, :, :]
        y = (lambda x, s, axes=(-2,-1): torch.fft.irfft2(x, s=s, dim=axes).to(torch.bfloat16))(Yf, (1, L2), axes=(-2, -1))[:, :, 0, :L]
        y = (lambda x, *d: x.permute(*d))(y, 0, 2, 1) + u * Dskip[None, None, :]
        return y

    def _B_ref(x, wqkv, wo):
        B, S, D = x.shape
        H = 32; Dh = D // H
        qkv = (lambda x, *d: x.permute(*d))((x @ wqkv).reshape(B, S, 3, H, Dh), 2, 0, 3, 1, 4)
        q, k, v = qkv[0], qkv[1], qkv[2]
        phi = lambda t: torch.where(t > 0, t + 1, torch.exp(t)) + 1e-6
        pq, pk = phi(q), phi(k)
        kv = torch.einsum("bhsd,bhse->bhde", pk, v)
        z = torch.einsum("bhsd->bhd", pk)
        num = torch.einsum("bhqd,bhde->bhqe", pq, kv)
        den = torch.einsum("bhqd,bhd->bhq", pq, z)[..., None] + 1e-6
        o = num / den
        return (lambda x, *d: x.permute(*d))(o, 0, 2, 1, 3).reshape(B, S, D) @ wo

    x = A_u
    g = _sigmoid((x * gate_w).sum(-1, keepdims=True))
    a = _A_ref(x, A_A_re, A_A_im, A_B_re, A_B_im, A_C_re, A_C_im, A_log_dt, A_Dskip)
    b = _B_ref(x, B_wqkv, B_wo)
    return g * a + (1 - g) * b
```

```cpp
#include <hip/hip_runtime.h>
#include <stdint.h>

typedef unsigned short u16;
typedef unsigned int u32;
typedef __attribute__((ext_vector_type(8))) __bf16 bf16x8;
typedef __attribute__((ext_vector_type(2))) __bf16 bf16x2;
typedef __attribute__((ext_vector_type(8))) _Float16 f16x8;
typedef __attribute__((ext_vector_type(2))) _Float16 f16x2;
typedef __attribute__((ext_vector_type(4))) short s16x4;
typedef __attribute__((ext_vector_type(4))) float f32x4;
typedef __attribute__((ext_vector_type(2))) float f32x2;
typedef __attribute__((ext_vector_type(4))) u32 u32x4;
typedef __attribute__((ext_vector_type(3))) u32 u32x3;
typedef __attribute__((ext_vector_type(2))) u32 u32x2;
#define LDSP __attribute__((address_space(3)))
#define DI __device__ __forceinline__

constexpr int SEQ = 2048, DM = 640, NW = 1920;

constexpr size_t WS_AQ = 0;
constexpr size_t WS_MW = WS_AQ + 128u * 20 * 1024;
constexpr size_t WS_YT = WS_MW + 40u * 20 * 1024;
constexpr size_t WS_GT = WS_YT + 640u * 2048 * 2;
constexpr size_t WS_KV = WS_GT + 2048u * 4;
constexpr int KVP_F4 = 336;
constexpr size_t WS_DBG = 64u << 20;
#define STAMP(ws, slot, role, i) do {} while (0)

DI float bf_lo(u32 v) { return __uint_as_float(v << 16); }
DI float bf_hi(u32 v) { return __uint_as_float(v & 0xffff0000u); }
DI float bf1(u16 v) { return __uint_as_float(((u32)v) << 16); }
DI u32 pk_bf(float a, float b) { f32x2 v = {a, b}; return __builtin_bit_cast(u32, __builtin_convertvector(v, bf16x2)); }
DI u32 pk_h(float a, float b) { f32x2 v = {a, b}; return __builtin_bit_cast(u32, __builtin_convertvector(v, f16x2)); }
DI bf16x8 as_bf8(u32x4 v) { return __builtin_bit_cast(bf16x8, v); }
DI f16x8 as_h8(u32x4 v) { return __builtin_bit_cast(f16x8, v); }
DI f32x4 mfma_bf(u32x4 a, u32x4 b, f32x4 c) { return __builtin_amdgcn_mfma_f32_16x16x32_bf16(as_bf8(a), as_bf8(b), c, 0, 0, 0); }
DI f32x4 mfma_h(u32x4 a, u32x4 b, f32x4 c) { return __builtin_amdgcn_mfma_f32_16x16x32_f16(as_h8(a), as_h8(b), c, 0, 0, 0); }
DI float phi(float t) { return (t > 0.f ? t + 1.f : __builtin_amdgcn_exp2f(t * 1.44269504089f)) + 1e-6f; }
DI void glds16(const void* g, char* l) {
  __builtin_amdgcn_global_load_lds((const __attribute__((address_space(1))) void*)g, (LDSP void*)l, 16, 0, 0);
}
DI void glds4(const char* sbase, u32 lds_dst, u32 o0, u32 o1, u32 o2, u32 o3) {
  u32 keep;
  asm volatile("s_mov_b32 %0, m0\n\ts_mov_b32 m0, %2\n\ts_nop 0\n\t"
               "global_load_lds_dwordx4 %3, %1\n\t"
               "global_load_lds_dwordx4 %4, %1 offset:1024\n\t"
               "global_load_lds_dwordx4 %5, %1 offset:2048\n\t"
               "global_load_lds_dwordx4 %6, %1 offset:3072\n\t"
               "s_mov_b32 m0, %0"
               : "=&s"(keep) : "s"(sbase), "s"(lds_dst), "v"(o0), "v"(o1), "v"(o2), "v"(o3) : "memory");
}
DI u32x2 tr_read(const char* l) {
  s16x4 t = __builtin_amdgcn_ds_read_tr16_b64_v4i16((LDSP s16x4*)l);
  return __builtin_bit_cast(u32x2, t);
}
#define WAIT_VM(n) asm volatile("s_waitcnt vmcnt(" #n ")" ::: "memory")

struct P1 {
  const u16 *u, *are, *aim, *bre, *bim, *cre, *cim, *logdt, *dskip, *wqkv, *gw;
  char* ws;
};

constexpr int C_UR0 = 0, C_UR1 = 4224, C_KN0 = 8448, C_KN1 = 13088, C_CH = 17920, C_SSTR = 544;

struct cpx { f32x2 r, i; };
DI cpx cmul(cpx a, cpx b) { cpx o; o.r = a.r * b.r - a.i * b.i; o.i = a.r * b.i + a.i * b.r; return o; }
DI cpx cexp_l(f32x2 er, f32x2 th, float l) {
  cpx o;
  f32x2 e = er * l, p = th * l;
  float m0 = __builtin_amdgcn_exp2f(e[0]), m1 = __builtin_amdgcn_exp2f(e[1]);
  float p0 = __builtin_amdgcn_fractf(p[0]), p1 = __builtin_amdgcn_fractf(p[1]);
  o.r[0] = m0 * __builtin_amdgcn_cosf(p0); o.r[1] = m1 * __builtin_amdgcn_cosf(p1);
  o.i[0] = m0 * __builtin_amdgcn_sinf(p0); o.i[1] = m1 * __builtin_amdgcn_sinf(p1);
  return o;
}
DI void put_row(char* srow, cpx v) {
  u32x2 w = {pk_h(v.r[0], v.i[0]), pk_h(v.r[1], v.i[1])};
  *(u32x2*)srow = w;
}
DI f32x4 kgen_tile(const char* sread, const u32x4 (&xf)[8]) {
  f32x4 acc = {0.f, 0.f, 0.f, 0.f};
#pragma unroll
  for (int s = 0; s < 8; ++s) acc = mfma_h(*(const u32x4*)(sread + 64 * s), xf[s], acc);
  return acc;
}

DI void role_conv(const P1& P, int cb, char* smem) {
  const int tid = threadIdx.x, lane = tid & 63;
  const int w = __builtin_amdgcn_readfirstlane(tid >> 6);
  const int d0 = cb * 4, d = d0 + w;
  char* ch = smem + w * C_CH;
  const int li = lane & 15, g = lane >> 4;

  STAMP(P.ws, blockIdx.x, 2, 1);
  u32x2 ur[9];
  {
    const u16* up = P.u + (size_t)(tid * 8) * DM + d0;
#pragma unroll
    for (int r = 0; r < 8; ++r) ur[r + 1] = *(const u32x2*)(up + r * DM);
    const u16* um = tid ? up - DM : up;
    ur[0] = *(const u32x2*)um;
  }
  const int pi = d * 128 + 2 * lane;
  u32 w_are = *(const u32*)(P.are + pi), w_aim = *(const u32*)(P.aim + pi);
  u32 w_bre = *(const u32*)(P.bre + pi), w_bim = *(const u32*)(P.bim + pi);
  u32 w_cre = *(const u32*)(P.cre + pi), w_cim = *(const u32*)(P.cim + pi);
  float dt = expf(bf1(P.logdt[d]));
  dt = bf_lo(pk_bf(dt, dt));
  const float dsk = bf1(P.dskip[d]);
  f32x2 er = {bf_lo(w_are), bf_hi(w_are)}, th = {bf_lo(w_aim), bf_hi(w_aim)};
  er = (er * dt) * 1.44269504089f;
  th = (th * dt) * 0.15915494309f;
  cpx B, C;
  B.r = f32x2{bf_lo(w_bre), bf_hi(w_bre)} * dt; B.i = f32x2{bf_lo(w_bim), bf_hi(w_bim)} * dt;
  C.r = f32x2{bf_lo(w_cre), bf_hi(w_cre)};      C.i = f32x2{bf_lo(w_cim), bf_hi(w_cim)};
  cpx c = cmul(C, B);
  float mx = fmaxf(fmaxf(fabsf(c.r[0]), fabsf(c.r[1])), fmaxf(fabsf(c.i[0]), fabsf(c.i[1])));
#pragma unroll
  for (int o = 32; o; o >>= 1) mx = fmaxf(mx, __shfl_xor(mx, o));
  mx = fmaxf(mx, 1e-30f);
  {
    float inv = 128.f / mx;
    c.r *= inv; c.i *= inv;
  }
  const float kscale = mx * (1.f / (128.f * 256.f));

  STAMP(P.ws, blockIdx.x, 2, 2);
  const cpx E1 = cexp_l(er, th, 1.f), E16 = cexp_l(er, th, 16.f), E256 = cexp_l(er, th, 256.f);
  char* sc = ch + C_KN0;
  char* swr = sc + lane * 8;
  const char* srd = sc + li * C_SSTR + g * 16;
  u32x4 xf[8];
  {
    cpx v; v.r = f32x2{256.f, 256.f}; v.i = f32x2{0.f, 0.f};
    cpx e = E16; e.i = -e.i;
#pragma unroll 4
    for (int o = 0; o < 16; ++o) { put_row(swr + o * C_SSTR, v); v = cmul(v, e); }
#pragma unroll
    for (int s = 0; s < 8; ++s) xf[s] = *(const u32x4*)(srd + 64 * s);
  }
  f32x4 k0, k1, k2;
  {
    cpx v = c;
#pragma unroll 4
    for (int o = 0; o < 16; ++o) { put_row(swr + o * C_SSTR, v); v = cmul(v, E1); }
    k0 = kgen_tile(srd, xf);
  }
  const cpx E2 = cmul(E1, E1), E4 = cmul(E2, E2), E8 = cmul(E4, E4);
  cpx G = cmul(c, E256);
  {
    cpx v = G;
#pragma unroll 4
    for (int o = 0; o < 8; ++o) { put_row(swr + o * C_SSTR, v); v = cmul(v, E2); }
#pragma unroll
    for (int m = 0; m < 2; ++m) {
      G = cmul(G, E256); v = G;
#pragma unroll
      for (int o = 0; o < 4; ++o) { put_row(swr + (8 + 4 * m + o) * C_SSTR, v); v = cmul(v, E4); }
    }
    k1 = kgen_tile(srd, xf);
  }
  {
#pragma unroll
    for (int m = 0; m < 4; ++m) {
      G = cmul(G, E256);
      put_row(swr + (2 * m) * C_SSTR, G);
      put_row(swr + (2 * m + 1) * C_SSTR, cmul(G, E8));
    }
    G = cmul(G, E256);
    put_row(swr + 8 * C_SSTR, G);
    k2 = kgen_tile(srd, xf);
  }
  k0 *= kscale; k1 *= kscale; k2 *= kscale;
  STAMP(P.ws, blockIdx.x, 2, 3);
  if (lane == 0) k0[0] += dsk;

  {
    char* a0 = ch + C_KN0 + 512;
    char* a1 = ch + C_KN1 + 512;
    u32x4 z4 = {0, 0, 0, 0};
    if (lane < 32) *(u32x4*)(ch + C_KN0 + lane * 16) = z4;
    else *(u32x4*)(ch + C_KN1 + (lane - 32) * 16) = z4;
    if (lane == 0) *(u32*)(a1) = 0;
    if (g < 2) {
#pragma unroll
      for (int pr = 0; pr < 2; ++pr) {
        const int l0 = 1024 + 256 * (2 * g + pr) + 16 * li;
        u32 a = pk_bf(k2[2 * pr], k2[2 * pr]), b = pk_bf(k2[2 * pr], k2[2 * pr + 1]), cc = pk_bf(k2[2 * pr + 1], k2[2 * pr + 1]);
        char* p0 = a0 + 2 * l0;
        *(u32x2*)(p0 - 8) = u32x2{a, a};
        *(u32x4*)(p0) = u32x4{a, a, b, cc};
        *(u32x2*)(p0 + 16) = u32x2{cc, cc};
        char* p1 = a1 + 2 * l0;
        *(u32*)(p1 - 4) = a;
        *(u32x4*)(p1) = u32x4{a, a, a, b};
        *(u32x2*)(p1 + 16) = u32x2{cc, cc};
        *(u32*)(p1 + 24) = cc;
      }
    } else {
      if (lane == 32) {
        u32 a = pk_bf(k2[0], k2[0]);
        *(u32x2*)(a0 + 2 * 2044) = u32x2{a, a};
        *(u32*)(a1 + 4 + 2 * 2044) = a;
        *(u32*)(a1 + 4 + 2 * 2046) = a;
      }
      const int l0 = 512 + 256 * (g - 2) + 16 * li;
      u32 v00 = pk_bf(k1[0], k1[0]), v01 = pk_bf(k1[0], k1[1]), v11 = pk_bf(k1[1], k1[1]);
      u32 v22 = pk_bf(k1[2], k1[2]), v23 = pk_bf(k1[2], k1[3]), v33 = pk_bf(k1[3], k1[3]);
      char* p0 = a0 + 2 * l0;
      *(u32*)(p0 - 4) = v00;
      *(u32x4*)(p0) = u32x4{v00, v01, v11, v22};
      *(u32x2*)(p0 + 16) = u32x2{v22, v23};
      *(u32*)(p0 + 24) = v33;
      char* p1 = a1 + 2 * l0;
      *(u32x4*)(p1) = u32x4{v00, v00, v01, v11};
      *(u32x4*)(p1 + 16) = u32x4{v22, v22, v23, v33};
    }
    if (g < 2) {
      const int l0 = 256 + 16 * li + 8 * g;
      u32 v00 = pk_bf(k1[0], k1[0]), v12 = pk_bf(k1[1], k1[2]), v22 = pk_bf(k1[2], k1[2]), v33 = pk_bf(k1[3], k1[3]);
      char* p0 = a0 + 2 * l0;
      *(u16*)(p0 - 2) = (u16)v00;
      *(u32x2*)(p0) = u32x2{v00, v12};
      *(u32*)(p0 + 8) = v22;
      *(u16*)(p0 + 12) = (u16)v33;
      char* p1 = a1 + 4 + 2 * l0;
      *(u16*)(p1 - 2) = (u16)v00;
      *(u32*)(p1) = v00;
      *(u32x2*)(p1 + 4) = u32x2{v12, v22};
      *(u16*)(p1 + 12) = (u16)v33;
    }
    {
      const int l0 = 16 * li + 4 * g;
      u32 v01 = pk_bf(k0[0], k0[1]), v23 = pk_bf(k0[2], k0[3]);
      *(u32x2*)(a0 + 2 * l0) = u32x2{v01, v23};
      *(u32*)(a1 + 4 + 2 * l0) = v01;
      *(u32*)(a1 + 8 + 2 * l0) = v23;
    }
  }
  STAMP(P.ws, blockIdx.x, 2, 4);
  {
    if (tid == 0) ur[0] = u32x2{0, 0};
    const int mo = (255 - tid) * 16;
#pragma unroll
    for (int cc = 0; cc < 4; ++cc) {
      const u32 sel = (cc & 1) ? 0x07060302u : 0x05040100u;
      u32x4 o1, o0;
#pragma unroll
      for (int q = 0; q < 4; ++q) {
        o1[q] = __builtin_amdgcn_perm(ur[7 - 2 * q][cc >> 1], ur[8 - 2 * q][cc >> 1], sel);
        o0[q] = __builtin_amdgcn_perm(ur[6 - 2 * q][cc >> 1], ur[7 - 2 * q][cc >> 1], sel);
      }
      *(u32x4*)(smem + cc * C_CH + C_UR1 + mo) = o1;
      *(u32x4*)(smem + cc * C_CH + C_UR0 + mo) = o0;
    }
    if (lane < 8) *(u32x4*)(ch + ((lane & 4) ? C_UR1 : C_UR0) + 4096 + (lane & 3) * 16) = u32x4{0, 0, 0, 0};
  }
  __syncthreads();

  STAMP(P.ws, blockIdx.x, 2, 5);
  f32x4 acc[8];
#pragma unroll
  for (int j = 0; j < 8; ++j) acc[j] = f32x4{0.f, 0.f, 0.f, 0.f};
  const char* pa = ch + ((li & 1) ? C_UR1 : C_UR0) + 4088 - 3584 - 8 * (li >> 1) + 16 * g;
  const char* pb = ch + ((li & 1) ? C_KN1 : C_KN0) + 512 + 64 * (li >> 1) + 16 * g;
#pragma unroll 1
  for (int dl = 0; dl < 8; ++dl) {
    u32x4 A[8], Bf[8];
#pragma unroll
    for (int m = 0; m < 8; ++m) {
      u32x2 lo = *(const u32x2*)(pa + 512 * (7 - m)), hi = *(const u32x2*)(pa + 512 * (7 - m) + 8);
      A[m] = u32x4{lo[0], lo[1], hi[0], hi[1]};
      Bf[m] = *(const u32x4*)(pb + 512 * m);
    }
#pragma unroll
    for (int J = 0; J < 8; ++J)
#pragma unroll
      for (int m = 0; m <= J; ++m) acc[J] = mfma_bf(A[m], Bf[J - m], acc[J]);
    pa -= 64; pb -= 64;
  }
  STAMP(P.ws, blockIdx.x, 2, 6);
  {
    u16* yt = (u16*)(P.ws + WS_YT) + (size_t)d * SEQ + 32 * (li >> 1) - 2 * (li & 1) + 8 * g + 2;
#pragma unroll
    for (int J = 0; J < 8; ++J) {
      *(u32*)(yt + 256 * J) = pk_bf(acc[J][0], acc[J][1]);
      *(u32*)(yt + 256 * J + 4) = pk_bf(acc[J][2], acc[J][3]);
    }
  }
  STAMP(P.ws, blockIdx.x, 2, 7);
}

DI void role_gemm(const P1& P, int hp, int p, char* smem) {
  const int tid = threadIdx.x, lane = tid & 63;
  const int w = __builtin_amdgcn_readfirstlane(tid >> 6);
  const int li = lane & 15, g = lane >> 4;
  const int t0 = 128 * p;
  STAMP(P.ws, blockIdx.x, 1, 1);
  u32 xo[4], wo_[4];
#pragma unroll
  for (int i = 0; i < 4; ++i) {
    int row = 32 * w + 8 * i + (lane >> 3);
    int c = (lane & 7) ^ ((row >> 1) & 7);
    xo[i] = ((t0 + row) * DM + 8 * c) * 2 + 4096 - 1024 * i;
    int n = 4 * w + i;
    int krow = 8 * (n >> 1) + ((lane >> 2) & 7);
    int chn = 4 * (2 * (n & 1) + (lane >> 5)) + ((lane & 3) ^ ((krow >> 2) & 3));
    int col;
    if (chn < 5) col = 640 + 8 * chn;
    else if (chn == 5) col = 1280 + 32;
    else if (chn < 10) col = 1280 + 8 * (chn - 6);
    else if (chn < 15) col = 8 * (chn - 10);
    else col = 32;
    wo_[i] = (krow * NW + col + 40 * hp) * 2 + 4096 - 1024 * i;
  }
  const u32 ldsb = (u32)(size_t)(LDSP char*)smem + w * 4096;
  auto issue = [&](int unit) {
    const u32 dst = ldsb + (unit % 5) * 16384;
    const int s = unit >> 1;
    if (unit & 1) glds4((const char*)P.wqkv - 4096 + (size_t)s * 128 * NW, dst, wo_[0], wo_[1], wo_[2], wo_[3]);
    else glds4((const char*)P.u - 4096 + s * 128, dst, xo[0], xo[1], xo[2], xo[3]);
  };
  f32x4 acc[2][8];
#pragma unroll
  for (int r = 0; r < 2; ++r)
#pragma unroll
    for (int c = 0; c < 8; ++c) acc[r][c] = f32x4{0.f, 0.f, 0.f, 0.f};
  const int xrd = (32 * w + li) * 128;
  const int xs0 = ((g) ^ ((li >> 1) & 7)) * 16, xs1 = ((4 + g) ^ ((li >> 1) & 7)) * 16;
  int wrd[2][2];
#pragma unroll
  for (int t = 0; t < 2; ++t)
#pragma unroll
    for (int cb = 0; cb < 2; ++cb) {
      int q = (lane & 15) >> 2, pp = lane & 3;
      wrd[t][cb] = 2048 * g + 64 * (4 * t + q) + 16 * ((2 * cb + (pp >> 1)) ^ ((2 * g + t) & 3)) + 8 * (pp & 1);
    }
  issue(0); issue(1); issue(2);
#pragma unroll
  for (int s = 0; s < 10; ++s) {
    if (s < 9) asm volatile("s_waitcnt vmcnt(4)\n\ts_barrier" ::: "memory");
    else asm volatile("s_waitcnt vmcnt(0)\n\ts_barrier" ::: "memory");
    if (2 * s + 3 < 20) issue(2 * s + 3);
    if (2 * s + 4 < 20) issue(2 * s + 4);
    const char* xb = smem + ((2 * s) % 5) * 16384 + xrd;
    const char* wb = smem + ((2 * s + 1) % 5) * 16384;
#pragma unroll
    for (int ks = 0; ks < 2; ++ks) {
      u32x4 a0 = *(const u32x4*)(xb + (ks ? xs1 : xs0));
      u32x4 a1 = *(const u32x4*)(xb + 2048 + (ks ? xs1 : xs0));
#pragma unroll
      for (int c = 0; c < 8; ++c) {
        u32x2 lo = tr_read(wb + ks * 8192 + 512 * (c >> 1) + wrd[0][c & 1]);
        u32x2 hi = tr_read(wb + ks * 8192 + 512 * (c >> 1) + wrd[1][c & 1]);
        u32x4 b = {lo[0], lo[1], hi[0], hi[1]};
        if (c < 5) {
          acc[0][c] = mfma_bf(a0, b, acc[0][c]);
          acc[1][c] = mfma_bf(a1, b, acc[1][c]);
        } else {
          acc[0][c] = mfma_bf(b, a0, acc[0][c]);
          acc[1][c] = mfma_bf(b, a1, acc[1][c]);
        }
      }
    }
    asm volatile("s_waitcnt lgkmcnt(0)" ::: "memory");
  }
  STAMP(P.ws, blockIdx.x, 1, 3);
  {
    char* aq = P.ws + WS_AQ;
#pragma unroll
    for (int r = 0; r < 2; ++r) {
      const int rt = 8 * p + 2 * w + r;
      u32x4 f = {pk_bf(phi(acc[r][5][0]), phi(acc[r][5][1])), pk_bf(phi(acc[r][5][2]), phi(acc[r][5][3])),
                 pk_bf(phi(acc[r][6][0]), phi(acc[r][6][1])), pk_bf(phi(acc[r][6][2]), phi(acc[r][6][3]))};
      *(u32x4*)(aq + ((size_t)(rt * 20 + hp) * 64 + lane) * 16) = f;
      u32x2 f2 = {pk_bf(phi(acc[r][7][0]), phi(acc[r][7][1])), pk_bf(phi(acc[r][7][2]), phi(acc[r][7][3]))};
      if (g < 2) *(u32x2*)(aq + ((size_t)(rt * 20 + 16 + (hp >> 2)) * 64 + li + 16 * (hp & 3)) * 16 + 8 * g) = f2;
    }
  }
  u32x4 fr[5];
#pragma unroll
  for (int c = 0; c < 5; ++c) {
    float v[8];
#pragma unroll
    for (int j = 0; j < 8; ++j) {
      float a = acc[j >> 2][c][j & 3];
      if (c < 2) a = phi(a);
      else if (c == 2) a = (li < 8) ? phi(a) : a;
      v[j] = a;
    }
    fr[c] = u32x4{pk_bf(v[0], v[1]), pk_bf(v[2], v[3]), pk_bf(v[4], v[5]), pk_bf(v[6], v[7])};
  }
  const f32x4 zero = {0.f, 0.f, 0.f, 0.f};
  const u32x4 ones = {0x3f803f80u, 0x3f803f80u, 0x3f803f80u, 0x3f803f80u};
  f32x4 s4 = mfma_bf(fr[3], fr[0], zero);
  f32x4 s5 = mfma_bf(fr[4], fr[1], zero);
  f32x4 k0v1 = mfma_bf(fr[4], fr[0], zero), k2v1 = mfma_bf(fr[4], fr[2], zero);
  f32x4 k1v0 = mfma_bf(fr[3], fr[1], zero), k1v2 = mfma_bf(fr[2], fr[1], zero);
  f32x4 s3 = mfma_bf(fr[2], fr[2], zero);
  f32x4 z0 = mfma_bf(fr[0], ones, zero), z1 = mfma_bf(fr[1], ones, zero), z2 = mfma_bf(fr[2], ones, zero);
  f32x4 s1 = (g == 0) ? k0v1 : k2v1;
  f32x4 s2 = (li < 4) ? k1v0 : k1v2;
  f32x4 zz = (li == 0) ? z0 : ((li == 1) ? z1 : z2);
  __syncthreads();
  {
    f32x4* red = (f32x4*)smem + w * KVP_F4;
    red[lane] = s1; red[64 + lane] = s2; red[128 + lane] = s3; red[192 + lane] = s4; red[256 + lane] = s5;
    if (li < 3) red[320 + li * 4 + g] = zz;
  }
  __syncthreads();
  {
    const f32x4* red = (const f32x4*)smem;
    f32x4* dst = (f32x4*)(P.ws + WS_KV) + (size_t)(hp * 16 + p) * KVP_F4;
    for (int i = tid; i < 332; i += 256)
      dst[i] = (red[i] + red[KVP_F4 + i]) + (red[2 * KVP_F4 + i] + red[3 * KVP_F4 + i]);
  }
  STAMP(P.ws, blockIdx.x, 1, 4);
}

DI void role_gate(const P1& P, int gb) {
  const int tid = threadIdx.x, lane = tid & 63, w = tid >> 6;
  const int li = lane & 15, g = lane >> 4;
  STAMP(P.ws, blockIdx.x, 3, 1);
  u32x4 gw[5], xv[4][5];
#pragma unroll
  for (int it = 0; it < 4; ++it) {
    const int row = gb * 64 + w * 16 + it * 4 + g;
#pragma unroll
    for (int c = 0; c < 5; ++c) xv[it][c] = *(const u32x4*)(P.u + (size_t)row * DM + (c * 16 + li) * 8);
  }
#pragma unroll
  for (int c = 0; c < 5; ++c) gw[c] = *(const u32x4*)(P.gw + (c * 16 + li) * 8);
  float* gt = (float*)(P.ws + WS_GT);
#pragma unroll
  for (int it = 0; it < 4; ++it) {
    float s = 0.f;
#pragma unroll
    for (int c = 0; c < 5; ++c)
#pragma unroll
      for (int j = 0; j < 4; ++j) s += bf_lo(xv[it][c][j]) * bf_lo(gw[c][j]) + bf_hi(xv[it][c][j]) * bf_hi(gw[c][j]);
#pragma unroll
    for (int o = 8; o; o >>= 1) s += __shfl_xor(s, o);
    if (li == 0) gt[gb * 64 + w * 16 + it * 4 + g] = 1.f / (1.f + __expf(-s));
  }
  STAMP(P.ws, blockIdx.x, 3, 2);
}

__global__ __launch_bounds__(256, 2) void k1(P1 P) {
  extern __shared__ __attribute__((aligned(16))) char smem[];
  const int b = blockIdx.x, x = b & 7, r = b >> 3;
  if (r < 32) role_gemm(P, (x & 1) * 8 + (r & 7), (x >> 1) * 4 + (r >> 3), smem);
  else if (r < 52) role_conv(P, x * 20 + (r - 32), smem);
  else role_gate(P, x * 4 + (r - 52));
}

__global__ __launch_bounds__(256) void kmid(const u16* wo, char* ws) {
  __shared__ __attribute__((aligned(16))) f32x4 red[4 * 320 + 16];
  const int tid = threadIdx.x, lane = tid & 63, w = tid >> 6, li = lane & 15, g = lane >> 4;
  const int b = blockIdx.x, x = b & 7, r_ = b >> 3;
  const int hp = (x & 1) * 8 + (r_ & 7), p = (x >> 1) * 4 + (r_ >> 3);
  const f32x4* kvp = (const f32x4*)(ws + WS_KV) + (size_t)hp * 16 * KVP_F4;
  STAMP(ws, 512 + blockIdx.x, 4, 1);
  if (tid < 192) {
    f32x4 v = kvp[(tid & 15) * KVP_F4 + 320 + (tid >> 4)];
#pragma unroll
    for (int o = 8; o; o >>= 1)
#pragma unroll
      for (int j = 0; j < 4; ++j) v[j] += __shfl_xor(v[j], o);
    if ((tid & 15) == 0) red[1280 + (tid >> 4)] = v;
  }
  if (p < 10) {
#pragma unroll
    for (int s = 0; s < 5; ++s) {
      f32x4 v = {0.f, 0.f, 0.f, 0.f};
#pragma unroll
      for (int q = 0; q < 4; ++q) v += kvp[(w * 4 + q) * KVP_F4 + s * 64 + lane];
      red[w * 320 + s * 64 + lane] = v;
    }
  }
  __syncthreads();
  const f32x4 zero = {0.f, 0.f, 0.f, 0.f};
  {
    f32x4 z0 = red[1280 + g], z1 = red[1284 + g], z2 = red[1288 + g];
    const int hsel = li & 3;
    u32x4 za, zb;
    za[0] = hsel == 0 ? pk_bf(z0[0], z0[1]) : 0; za[1] = hsel == 0 ? pk_bf(z0[2], z0[3]) : 0;
    const bool m1 = hsel == (g == 0 ? 0 : 1);
    za[2] = m1 ? pk_bf(z1[0], z1[1]) : 0; za[3] = m1 ? pk_bf(z1[2], z1[3]) : 0;
    const bool m2 = hsel == 1 && g < 2;
    zb[0] = m2 ? pk_bf(z2[0], z2[1]) : 0; zb[1] = m2 ? pk_bf(z2[2], z2[3]) : 0; zb[2] = 0; zb[3] = 0;
    char* aq = ws + WS_AQ;
#pragma unroll
    for (int r = 0; r < 2; ++r) {
      const int rt = 8 * p + 2 * w + r;
      u32x4* pf = (u32x4*)(aq + ((size_t)(rt * 20 + hp) * 64 + lane) * 16);
      u32x2* pf2 = (u32x2*)(aq + ((size_t)(rt * 20 + 16 + (hp >> 2)) * 64 + li + 16 * (hp & 3)) * 16 + 8 * (g & 1));
      u32x4 f = *pf;
      u32x2 f2 = *pf2;
      u32x4 f2e = {g < 2 ? f2[0] : 0u, g < 2 ? f2[1] : 0u, 0u, 0u};
      f32x4 den = mfma_bf(za, f, zero);
      den = mfma_bf(zb, f2e, den);
      const float ra = 1.f / (den[0] + 1e-6f), rb = 1.f / (den[1] + 1e-6f);
      const float r1 = g == 0 ? ra : rb;
      u32x4 o = {pk_bf(bf_lo(f[0]) * ra, bf_hi(f[0]) * ra), pk_bf(bf_lo(f[1]) * ra, bf_hi(f[1]) * ra),
                 pk_bf(bf_lo(f[2]) * r1, bf_hi(f[2]) * r1), pk_bf(bf_lo(f[3]) * r1, bf_hi(f[3]) * r1)};
      *pf = o;
      u32x2 o2 = {pk_bf(bf_lo(f2[0]) * rb, bf_hi(f2[0]) * rb), pk_bf(bf_lo(f2[1]) * rb, bf_hi(f2[1]) * rb)};
      if (g < 2) *pf2 = o2;
    }
  }
  if (p < 10) {
    f32x4 s[5];
#pragma unroll
    for (int q = 0; q < 5; ++q) s[q] = (red[q * 64 + lane] + red[320 + q * 64 + lane]) + (red[640 + q * 64 + lane] + red[960 + q * 64 + lane]);
    u32x2 sp[5];
#pragma unroll
    for (int q = 0; q < 5; ++q) sp[q] = u32x2{pk_bf(s[q][0], s[q][1]), pk_bf(s[q][2], s[q][3])};
    const u32x2 z2 = {0u, 0u};
    u32x2 k0lo = sp[3], k0hi = g == 0 ? sp[0] : z2;
    u32x2 k1lo = li < 4 ? sp[1] : z2, k1hi = ((li < 4) == (g == 0)) ? sp[4] : z2;
    u32x2 k1b = (li >= 4 && g >= 2) ? sp[1] : z2;
    u32x2 k2hi = (g >= 1 && li < 8) ? sp[0] : z2;
    u32x2 k2b = (g >= 2 && li < 8) ? sp[2] : z2;
    const int ct = 4 * p + w;
    const u16* wp = wo + (size_t)(40 * hp + 4 * g) * DM + 16 * ct + li;
    u32 e[12];
#pragma unroll
    for (int j = 0; j < 4; ++j) {
      e[j] = wp[j * DM];
      e[4 + j] = wp[(16 + j) * DM];
      e[8 + j] = g >= 2 ? (u32)wp[(24 + j) * DM] : 0u;
    }
    u32x4 b0 = {e[0] | (e[1] << 16), e[2] | (e[3] << 16), e[4] | (e[5] << 16), e[6] | (e[7] << 16)};
    u32x4 b1 = {e[8] | (e[9] << 16), e[10] | (e[11] << 16), 0u, 0u};
    f32x4 m0 = mfma_bf(u32x4{k0lo[0], k0lo[1], k0hi[0], k0hi[1]}, b0, zero);
    f32x4 m1 = mfma_bf(u32x4{k1lo[0], k1lo[1], k1hi[0], k1hi[1]}, b0, zero);
    m1 = mfma_bf(u32x4{k1b[0], k1b[1], 0u, 0u}, b1, m1);
    f32x4 m2 = mfma_bf(u32x4{0u, 0u, k2hi[0], k2hi[1]}, b0, zero);
    m2 = mfma_bf(u32x4{k2b[0], k2b[1], 0u, 0u}, b1, m2);
    char* mw = ws + WS_MW;
    *(u32x4*)(mw + ((size_t)(ct * 20 + hp) * 64 + lane) * 16) =
        u32x4{pk_bf(m0[0], m0[1]), pk_bf(m0[2], m0[3]), pk_bf(m1[0], m1[1]), pk_bf(m1[2], m1[3])};
    if (g < 2)
      *(u32x2*)(mw + ((size_t)(ct * 20 + 16 + (hp >> 2)) * 64 + li + 16 * (hp & 3)) * 16 + 8 * g) =
          u32x2{pk_bf(m2[0], m2[1]), pk_bf(m2[2], m2[3])};
  }
  STAMP(ws, 512 + blockIdx.x, 4, 2);
}

__global__ __launch_bounds__(256) void k2(const char* ws, u16* out) {
  extern __shared__ __attribute__((aligned(16))) char smem[];
  const int tid = threadIdx.x, lane = tid & 63, li = lane & 15, g = lane >> 4;
  const int w = __builtin_amdgcn_readfirstlane(tid >> 6);
  const int b = blockIdx.x, x = b & 7, r_ = b >> 3;
  const int rg = (x >> 1) * 8 + (r_ >> 2), cg = (x & 1) * 4 + (r_ & 3);
  STAMP(ws, 1024 + blockIdx.x, 5, 1);
  const char* msrc = ws + WS_MW + (size_t)cg * 5 * 20480 + lane * 16;
#pragma unroll
  for (int i = 0; i < 25; ++i) glds16(msrc + (i * 4 + w) * 1024, smem + (i * 4 + w) * 1024);
  const int rt = rg * 4 + w;
  const u32x4* asrc = (const u32x4*)(ws + WS_AQ + (size_t)rt * 20480) + lane;
  u32x4 a[20];
#pragma unroll
  for (int s = 0; s < 20; ++s) a[s] = asrc[s * 64];
  const int t0 = rt * 16 + 4 * g;
  u32x2 ya[5];
#pragma unroll
  for (int c = 0; c < 5; ++c) ya[c] = *(const u32x2*)((const u16*)(ws + WS_YT) + (size_t)(cg * 80 + c * 16 + li) * SEQ + t0);
  const f32x4 gt = *(const f32x4*)((const float*)(ws + WS_GT) + t0);
  WAIT_VM(0);
  __syncthreads();
  STAMP(ws, 1024 + blockIdx.x, 5, 2);
  f32x4 acc[5];
#pragma unroll
  for (int c = 0; c < 5; ++c) acc[c] = f32x4{0.f, 0.f, 0.f, 0.f};
#pragma unroll
  for (int s = 0; s < 20; ++s)
#pragma unroll
    for (int c = 0; c < 5; ++c)
      acc[c] = mfma_bf(a[s], *(const u32x4*)(smem + (c * 20 + s) * 1024 + lane * 16), acc[c]);
  STAMP(ws, 1024 + blockIdx.x, 5, 3);
  const bool odd = li & 1;
  u16* op = out + (size_t)(t0 + (odd ? 2 : 0)) * DM + cg * 80 + (li & ~1);
#pragma unroll
  for (int c = 0; c < 5; ++c) {
    float v0 = gt[0] * bf_lo(ya[c][0]) + (1.f - gt[0]) * acc[c][0];
    float v1 = gt[1] * bf_hi(ya[c][0]) + (1.f - gt[1]) * acc[c][1];
    float v2 = gt[2] * bf_lo(ya[c][1]) + (1.f - gt[2]) * acc[c][2];
    float v3 = gt[3] * bf_hi(ya[c][1]) + (1.f - gt[3]) * acc[c][3];
    u32 p01 = pk_bf(v0, v1), p23 = pk_bf(v2, v3);
    u32 send = odd ? p01 : p23, keep = odd ? p23 : p01;
    u32 recv = __builtin_amdgcn_mov_dpp(send, 0xB1, 0xf, 0xf, true);
    u32 lo = odd ? recv : keep, hi = odd ? keep : recv;
    *(u32*)(op + c * 16) = __builtin_amdgcn_perm(hi, lo, 0x05040100u);
    *(u32*)(op + c * 16 + DM) = __builtin_amdgcn_perm(hi, lo, 0x07060302u);
  }
  STAMP(ws, 1024 + blockIdx.x, 5, 4);
}

extern "C" void kernel_launch(void* const* d_in, const int* in_sizes, int n_in, void* d_out, int out_size,
                              void* d_ws, size_t ws_size, hipStream_t stream) {
  P1 P;
  P.u = (const u16*)d_in[0]; P.are = (const u16*)d_in[1]; P.aim = (const u16*)d_in[2];
  P.bre = (const u16*)d_in[3]; P.bim = (const u16*)d_in[4]; P.cre = (const u16*)d_in[5]; P.cim = (const u16*)d_in[6];
  P.logdt = (const u16*)d_in[7]; P.dskip = (const u16*)d_in[8]; P.wqkv = (const u16*)d_in[10];
  P.gw = (const u16*)d_in[12]; P.ws = (char*)d_ws;
  (void)hipFuncSetAttribute((const void*)k1, hipFuncAttributeMaxDynamicSharedMemorySize, 81920);
  (void)hipFuncSetAttribute((const void*)k2, hipFuncAttributeMaxDynamicSharedMemorySize, 102400);
  k1<<<448, 256, 81920, stream>>>(P);
  kmid<<<256, 256, 0, stream>>>((const u16*)d_in[11], (char*)d_ws);
  k2<<<256, 256, 102400, stream>>>((const char*)d_ws, (u16*)d_out);
}
```

```cpp
#include <hip/hip_runtime.h>
#include <stdint.h>

typedef unsigned short u16;
typedef unsigned int u32;
typedef unsigned long long u64;
typedef __attribute__((ext_vector_type(8))) __bf16 bf16x8;
typedef __attribute__((ext_vector_type(2))) __bf16 bf16x2;
typedef __attribute__((ext_vector_type(8))) _Float16 f16x8;
typedef __attribute__((ext_vector_type(2))) _Float16 f16x2;
typedef __attribute__((ext_vector_type(4))) short s16x4;
typedef __attribute__((ext_vector_type(4))) float f32x4;
typedef __attribute__((ext_vector_type(2))) float f32x2;
typedef __attribute__((ext_vector_type(4))) u32 u32x4;
typedef __attribute__((ext_vector_type(3))) u32 u32x3;
typedef __attribute__((ext_vector_type(2))) u32 u32x2;
#define LDSP __attribute__((address_space(3)))
#define GLBP __attribute__((address_space(1)))
#define DI __device__ __forceinline__

constexpr int SEQ = 2048, DM = 640, NW = 1920;

constexpr u32 WS_AQ = 0;
constexpr u32 WS_MW = WS_AQ + 128u * 20 * 1024;
constexpr u32 WS_YT = WS_MW + 40u * 20 * 1024;
constexpr u32 WS_GT = WS_YT + 640u * 2048 * 2;
constexpr u32 WS_KV = WS_GT + 2048u * 4;
constexpr u32 WS_ZP = WS_KV + 32u * 100 * 256;
constexpr u32 WS_EP = WS_ZP + 8u * 20 * 256;
constexpr u32 WS_END = WS_EP + 8u * 64;
constexpr u64 EPOCH_STEP = 0x9E37001001001001ull;
constexpr u32 SPIN_MAX = 200000;

constexpr size_t WS_DBG = 64u << 20;
#define STAMP(ws, slot, role, i) do {} while (0)

DI float bf_lo(u32 v) { return __uint_as_float(v << 16); }
DI float bf_hi(u32 v) { return __uint_as_float(v & 0xffff0000u); }
DI float bf1(u16 v) { return __uint_as_float(((u32)v) << 16); }
DI u32 pk_bf(float a, float b) { f32x2 v = {a, b}; return __builtin_bit_cast(u32, __builtin_convertvector(v, bf16x2)); }
DI u32 pk_h(float a, float b) { f32x2 v = {a, b}; return __builtin_bit_cast(u32, __builtin_convertvector(v, f16x2)); }
DI u32x2 pk4_bf(f32x4 v) { return u32x2{pk_bf(v[0], v[1]), pk_bf(v[2], v[3])}; }
DI bf16x8 as_bf8(u32x4 v) { return __builtin_bit_cast(bf16x8, v); }
DI f16x8 as_h8(u32x4 v) { return __builtin_bit_cast(f16x8, v); }
DI u32x4 cat(u32x2 a, u32x2 b) { return u32x4{a[0], a[1], b[0], b[1]}; }
DI f32x4 mfma_bf(u32x4 a, u32x4 b, f32x4 c) { return __builtin_amdgcn_mfma_f32_16x16x32_bf16(as_bf8(a), as_bf8(b), c, 0, 0, 0); }
DI f32x4 mfma_h(u32x4 a, u32x4 b, f32x4 c) { return __builtin_amdgcn_mfma_f32_16x16x32_f16(as_h8(a), as_h8(b), c, 0, 0, 0); }
DI float phi(float t) { return (t > 0.f ? t + 1.f : __builtin_amdgcn_exp2f(t * 1.44269504089f)) + 1e-6f; }
DI f32x4 phi4(f32x4 v) { return f32x4{phi(v[0]), phi(v[1]), phi(v[2]), phi(v[3])}; }
DI u32x2 tr_read(const char* l) {
  s16x4 t = __builtin_amdgcn_ds_read_tr16_b64_v4i16((LDSP s16x4*)l);
  return __builtin_bit_cast(u32x2, t);
}
template <int CTRL> DI float dpp_add(float v) {
  return v + __int_as_float(__builtin_amdgcn_mov_dpp(__float_as_int(v), CTRL, 0xf, 0xf, true));
}
template <int N> struct IC { static constexpr int value = N; };
#define SCHED() __builtin_amdgcn_sched_barrier(0)
#define WAIT_LGKM() asm volatile("s_waitcnt lgkmcnt(0)" ::: "memory")
template <int N> DI void wait_vm() { asm volatile("s_waitcnt vmcnt(%0)" ::"n"(N) : "memory"); }
template <int N> DI void wait_vm_bar() { asm volatile("s_waitcnt vmcnt(%0) lgkmcnt(0)\n\ts_barrier" ::"n"(N) : "memory"); }

DI void glds1(const char* sbase, u32 dst, u32 off) {
  u32 keep;
  asm volatile("s_mov_b32 %0, m0\n\ts_mov_b32 m0, %2\n\ts_nop 0\n\t"
               "global_load_lds_dwordx4 %3, %1\n\t"
               "s_mov_b32 m0, %0"
               : "=&s"(keep) : "s"(sbase), "s"(dst), "v"(off) : "memory");
}
typedef __amdgpu_buffer_rsrc_t rsrc_t;
DI rsrc_t ws_rsrc(char* ws) { return __builtin_amdgcn_make_buffer_rsrc((void*)ws, (short)0, (int)WS_END, 0x00020000); }
DI void st16_dev(rsrc_t r, u32 off, u32x4 v) { __builtin_amdgcn_raw_buffer_store_b128(v, r, off, 0, 16); }
#define WT_AUX 16
DI void st16_wt(rsrc_t r, u32 off, u32x4 v) { __builtin_amdgcn_raw_buffer_store_b128(v, r, off, 0, WT_AUX); }
DI void st8_wt(rsrc_t r, u32 off, u32x2 v) { __builtin_amdgcn_raw_buffer_store_b64(v, r, off, 0, WT_AUX); }
DI void st4_dev(rsrc_t r, u32 off, u32 v) { __builtin_amdgcn_raw_buffer_store_b32(v, r, off, 0, 16); }
DI u32x4 ld16_dev(rsrc_t r, u32 off) { return __builtin_amdgcn_raw_buffer_load_b128(r, off, 0, 16); }
DI void st8_dev(char* p, u64 v) { __hip_atomic_store((GLBP u64*)p, v, __ATOMIC_RELAXED, __HIP_MEMORY_SCOPE_AGENT); }
DI u64 ld8_dev(const char* p) { return __hip_atomic_load((GLBP u64*)p, __ATOMIC_RELAXED, __HIP_MEMORY_SCOPE_AGENT); }
struct Tag { u32 t0, t1, t2, t3; };
DI Tag mk_tag(u64 t) { Tag g; g.t0 = (u32)t & 0xfff; g.t1 = (u32)(t >> 12) & 0xfff; g.t2 = (u32)(t >> 24) & 0xfff; g.t3 = (u32)(t >> 36) & 0xfff; return g; }
DI u32x4 tag4(f32x4 v, Tag t) {
  u32x4 b = __builtin_bit_cast(u32x4, v);
  return u32x4{((b[0] + 0x800u) & 0xfffff000u) | t.t0, ((b[1] + 0x800u) & 0xfffff000u) | t.t1,
               ((b[2] + 0x800u) & 0xfffff000u) | t.t2, ((b[3] + 0x800u) & 0xfffff000u) | t.t3};
}
DI u32 tag_bad(u32x4 b, Tag t) { return (b[0] ^ t.t0) | (b[1] ^ t.t1) | (b[2] ^ t.t2) | (b[3] ^ t.t3); }
DI f32x4 untag(u32x4 b) { return __builtin_bit_cast(f32x4, b & 0xfffff000u); }

DI u64 next_tag(const char* p) {
  u64 e;
  asm volatile("s_load_dwordx2 %0, %1, 0x0\n\ts_waitcnt lgkmcnt(0)" : "=s"(e) : "s"(p) : "memory");
  return e + EPOCH_STEP;
}

struct P1 {
  const u16 *u, *are, *aim, *bre, *bim, *cre, *cim, *logdt, *dskip, *wqkv, *wo, *gw;
  char* ws;
};

constexpr int C_UR0 = 256, C_UR1 = 4480, C_KN0 = 8704, C_KN1 = 13344, C_CH = 18176, C_SSTR = 544;

struct cpx { float r0, r1, i0, i1; };
DI float vmul(float a, float b) { float d; asm("v_mul_f32 %0, %1, %2" : "=v"(d) : "v"(a), "v"(b)); return d; }
DI float vfma(float a, float b, float c) { float d; asm("v_fma_f32 %0, %1, %2, %3" : "=v"(d) : "v"(a), "v"(b), "v"(c)); return d; }
DI float vfms(float a, float b, float c) { float d; asm("v_fma_f32 %0, %1, %2, -%3" : "=v"(d) : "v"(a), "v"(b), "v"(c)); return d; }
DI cpx cmul(cpx a, cpx b) {
  cpx o;
  f32x2 ar = {a.r0, a.r1}, ai = {a.i0, a.i1}, br = {b.r0, b.r1}, bi = {b.i0, b.i1};
  f32x2 r = ar * br - ai * bi, i = ar * bi + ai * br;
  o.r0 = r[0]; o.r1 = r[1]; o.i0 = i[0]; o.i1 = i[1];
  return o;
  o.r0 = vfms(a.r0, b.r0, vmul(a.i0, b.i0)); o.i0 = vfma(a.r0, b.i0, vmul(a.i0, b.r0));
  o.r1 = vfms(a.r1, b.r1, vmul(a.i1, b.i1)); o.i1 = vfma(a.r1, b.i1, vmul(a.i1, b.r1));
  return o;
}
DI cpx cexp_l(f32x2 er, f32x2 th, float l) {
  cpx o;
  float m0 = __builtin_amdgcn_exp2f(er[0] * l), m1 = __builtin_amdgcn_exp2f(er[1] * l);
  float p0 = __builtin_amdgcn_fractf(th[0] * l), p1 = __builtin_amdgcn_fractf(th[1] * l);
  o.r0 = m0 * __builtin_amdgcn_cosf(p0); o.r1 = m1 * __builtin_amdgcn_cosf(p1);
  o.i0 = m0 * __builtin_amdgcn_sinf(p0); o.i1 = m1 * __builtin_amdgcn_sinf(p1);
  return o;
}
DI void put_row(char* srow, cpx v) {
  u32x2 w = {pk_h(v.r0, v.i0), pk_h(v.r1, v.i1)};
  *(u32x2*)srow = w;
}
DI f32x4 kgen_tile(const char* sread, const u32x4 (&xf)[8]) {
  f32x4 acc = {0.f, 0.f, 0.f, 0.f};
#pragma unroll
  for (int s = 0; s < 8; ++s) acc = mfma_h(*(const u32x4*)(sread + 64 * s), xf[s], acc);
  return acc;
}

DI void role_conv(const P1& P, int cb, char* smem) {
  const int tid = threadIdx.x, lane = tid & 63;
  const int w = __builtin_amdgcn_readfirstlane(tid >> 6);
  const int d0 = cb * 8, d = d0 + w;
  char* ch = smem + w * C_CH;
  const int li = lane & 15, g = lane >> 4;
  STAMP(P.ws, blockIdx.x, 2, 1);
  const int pi = d * 128 + 2 * lane;
  u32 w_are = *(const u32*)(P.are + pi), w_aim = *(const u32*)(P.aim + pi);
  u32 w_bre = *(const u32*)(P.bre + pi), w_bim = *(const u32*)(P.bim + pi);
  u32 w_cre = *(const u32*)(P.cre + pi), w_cim = *(const u32*)(P.cim + pi);
  const u16 w_ldt = P.logdt[d], w_dsk = P.dskip[d];
  SCHED();
  u32x4 ur[5];
  {
    const u16* up = P.u + (size_t)(tid * 4) * DM + d0;
#pragma unroll
    for (int r = 0; r < 4; ++r) ur[r + 1] = *(const u32x4*)(up + r * DM);
    ur[0] = *(const u32x4*)(tid ? up - DM : up);
  }
  SCHED();
  float dt = expf(bf1(w_ldt));
  const float dsk = bf1(w_dsk);
  dt = bf_lo(pk_bf(dt, dt));
  f32x2 er = {bf_lo(w_are), bf_hi(w_are)}, th = {bf_lo(w_aim), bf_hi(w_aim)};
  er = (er * dt) * 1.44269504089f;
  th = (th * dt) * 0.15915494309f;
  cpx B, C;
  B.r0 = bf_lo(w_bre) * dt; B.r1 = bf_hi(w_bre) * dt; B.i0 = bf_lo(w_bim) * dt; B.i1 = bf_hi(w_bim) * dt;
  C.r0 = bf_lo(w_cre); C.r1 = bf_hi(w_cre); C.i0 = bf_lo(w_cim); C.i1 = bf_hi(w_cim);
  cpx c = cmul(C, B);
  float mx = fmaxf(fmaxf(fabsf(c.r0), fabsf(c.r1)), fmaxf(fabsf(c.i0), fabsf(c.i1)));
#pragma unroll
  for (int o = 32; o; o >>= 1) mx = fmaxf(mx, __shfl_xor(mx, o));
  const u32 mexp = min(max(__float_as_uint(mx) & 0x7f800000u, 0x0c000000u), 0x7c000000u);
  {
    const float inv = __uint_as_float(0x82000000u - mexp);
    c.r0 *= inv; c.r1 *= inv; c.i0 *= inv; c.i1 *= inv;
  }
  const float kscale = __uint_as_float(mexp - 0x07000000u);
  STAMP(P.ws, blockIdx.x, 2, 2);

  const cpx E1 = cexp_l(er, th, 1.f), E16 = cexp_l(er, th, 16.f), E256 = cexp_l(er, th, 256.f);
  char* sc = ch + C_KN0;
  char* swr = sc + lane * 8;
  const char* srd = sc + li * C_SSTR + g * 16;
  u32x4 xf[8];
  {
    cpx v; v.r0 = 256.f; v.r1 = 256.f; v.i0 = 0.f; v.i1 = 0.f;
    cpx e = E16; e.i0 = -e.i0; e.i1 = -e.i1;
#pragma unroll 4
    for (int o = 0; o < 16; ++o) { put_row(swr + o * C_SSTR, v); v = cmul(v, e); }
#pragma unroll
    for (int s = 0; s < 8; ++s) xf[s] = *(const u32x4*)(srd + 64 * s);
  }
  f32x4 k0, k1, k2;
  {
    cpx v = c;
#pragma unroll 4
    for (int o = 0; o < 16; ++o) { put_row(swr + o * C_SSTR, v); v = cmul(v, E1); }
    k0 = kgen_tile(srd, xf);
  }
  const cpx E2 = cmul(E1, E1), E4 = cmul(E2, E2), E8 = cmul(E4, E4);
  cpx G = cmul(c, E256);
  {
    cpx v = G;
#pragma unroll 4
    for (int o = 0; o < 8; ++o) { put_row(swr + o * C_SSTR, v); v = cmul(v, E2); }
#pragma unroll
    for (int m = 0; m < 2; ++m) {
      G = cmul(G, E256); v = G;
#pragma unroll
      for (int o = 0; o < 4; ++o) { put_row(swr + (8 + 4 * m + o) * C_SSTR, v); v = cmul(v, E4); }
    }
    k1 = kgen_tile(srd, xf);
  }
  const rsrc_t rs = ws_rsrc(P.ws);
  {
#pragma unroll
    for (int m = 0; m < 4; ++m) {
      G = cmul(G, E256);
      put_row(swr + (2 * m) * C_SSTR, G);
      put_row(swr + (2 * m + 1) * C_SSTR, cmul(G, E8));
    }
    G = cmul(G, E256);
    put_row(swr + 8 * C_SSTR, G);
    k2 = kgen_tile(srd, xf);
  }
  k0 *= kscale; k1 *= kscale; k2 *= kscale;
  STAMP(P.ws, blockIdx.x, 2, 3);
  if (lane == 0) k0[0] += dsk;

  {
    char* a0 = ch + C_KN0 + 512;
    char* a1 = ch + C_KN1 + 512;
    u32x4 z4 = {0, 0, 0, 0};
    *(u32x4*)(ch + (lane < 32 ? C_KN0 : C_KN1 - 512) + lane * 16) = z4;
    if (lane < 8) *(u32x4*)(ch + ((lane & 4) ? C_UR1 : C_UR0) + 4096 + (lane & 3) * 16) = z4;
    if (lane == 0) *(u32*)(a1) = 0;
    if (g < 2) {
#pragma unroll
      for (int pr = 0; pr < 2; ++pr) {
        const int l0 = 1024 + 256 * (2 * g + pr) + 16 * li;
        u32 a = pk_bf(k2[2 * pr], k2[2 * pr]), b = pk_bf(k2[2 * pr], k2[2 * pr + 1]), cc = pk_bf(k2[2 * pr + 1], k2[2 * pr + 1]);
        char* p0 = a0 + 2 * l0;
        *(u32x2*)(p0 - 8) = u32x2{a, a};
        *(u32x4*)(p0) = u32x4{a, a, b, cc};
        *(u32x2*)(p0 + 16) = u32x2{cc, cc};
        char* p1 = a1 + 2 * l0;
        *(u32*)(p1 - 4) = a;
        *(u32x4*)(p1) = u32x4{a, a, a, b};
        *(u32x2*)(p1 + 16) = u32x2{cc, cc};
        *(u32*)(p1 + 24) = cc;
      }
    } else {
      if (lane == 32) {
        u32 a = pk_bf(k2[0], k2[0]);
        *(u32x2*)(a0 + 2 * 2044) = u32x2{a, a};
        *(u32*)(a1 + 4 + 2 * 2044) = a;
        *(u32*)(a1 + 4 + 2 * 2046) = a;
      }
      const int l0 = 512 + 256 * (g - 2) + 16 * li;
      u32 v00 = pk_bf(k1[0], k1[0]), v01 = pk_bf(k1[0], k1[1]), v11 = pk_bf(k1[1], k1[1]);
      u32 v22 = pk_bf(k1[2], k1[2]), v23 = pk_bf(k1[2], k1[3]), v33 = pk_bf(k1[3], k1[3]);
      char* p0 = a0 + 2 * l0;
      *(u32*)(p0 - 4) = v00;
      *(u32x4*)(p0) = u32x4{v00, v01, v11, v22};
      *(u32x2*)(p0 + 16) = u32x2{v22, v23};
      *(u32*)(p0 + 24) = v33;
      char* p1 = a1 + 2 * l0;
      *(u32x4*)(p1) = u32x4{v00, v00, v01, v11};
      *(u32x4*)(p1 + 16) = u32x4{v22, v22, v23, v33};
    }
    if (g < 2) {
      const int l0 = 256 + 16 * li + 8 * g;
      u32 v00 = pk_bf(k1[0], k1[0]), v12 = pk_bf(k1[1], k1[2]), v22 = pk_bf(k1[2], k1[2]), v33 = pk_bf(k1[3], k1[3]);
      char* p0 = a0 + 2 * l0;
      *(u16*)(p0 - 2) = (u16)v00;
      *(u32x2*)(p0) = u32x2{v00, v12};
      *(u32*)(p0 + 8) = v22;
      *(u16*)(p0 + 12) = (u16)v33;
      char* p1 = a1 + 4 + 2 * l0;
      *(u16*)(p1 - 2) = (u16)v00;
      *(u32*)(p1) = v00;
      *(u32x2*)(p1 + 4) = u32x2{v12, v22};
      *(u16*)(p1 + 12) = (u16)v33;
    }
    {
      const int l0 = 16 * li + 4 * g;
      u32 v01 = pk_bf(k0[0], k0[1]), v23 = pk_bf(k0[2], k0[3]);
      *(u32x2*)(a0 + 2 * l0) = u32x2{v01, v23};
      *(u32*)(a1 + 4 + 2 * l0) = v01;
      *(u32*)(a1 + 8 + 2 * l0) = v23;
    }
  }
  STAMP(P.ws, blockIdx.x, 2, 4);
  {
    if (tid == 0) ur[0] = u32x4{0, 0, 0, 0};
    char* ub = smem + 4088 - 8 * tid;
#pragma unroll
    for (int cc = 0; cc < 8; ++cc) {
      const u32 sel = (cc & 1) ? 0x07060302u : 0x05040100u;
      const int k = cc >> 1;
      *(u32x2*)(ub + cc * C_CH + C_UR1) = u32x2{__builtin_amdgcn_perm(ur[3][k], ur[4][k], sel), __builtin_amdgcn_perm(ur[1][k], ur[2][k], sel)};
      *(u32x2*)(ub + cc * C_CH + C_UR0) = u32x2{__builtin_amdgcn_perm(ur[2][k], ur[3][k], sel), __builtin_amdgcn_perm(ur[0][k], ur[1][k], sel)};
    }
  }
  __syncthreads();
  STAMP(P.ws, blockIdx.x, 2, 6);

  f32x4 acc[8];
#pragma unroll
  for (int j = 0; j < 8; ++j) acc[j] = f32x4{0.f, 0.f, 0.f, 0.f};
  const char* pa = ch + ((li & 1) ? C_UR1 : C_UR0) + 4088 - 3584 - 8 * (li >> 1) + 16 * g;
  const LDSP char* pa2 = (const LDSP char*)pa + 8;
  asm volatile("" : "+v"(pa2));
  const char* pb = ch + ((li & 1) ? C_KN1 : C_KN0) + 512 + 64 * (li >> 1) + 16 * g;
  u32x4 A[8], Bf[8];
#pragma unroll
  for (int m = 0; m < 8; ++m) {
    A[m] = cat(*(const u32x2*)(pa + 512 * (7 - m)), *(const LDSP u32x2*)(pa2 + 512 * (7 - m)));
    Bf[7 - m] = *(const u32x4*)(pb + 512 * (7 - m));
    SCHED();
  }
  auto ldA0 = [&](int m, int o) { u32x2 v = *(const u32x2*)(pa + 512 * (7 - m) + o); A[m][0] = v[0]; A[m][1] = v[1]; };
  auto ldA1 = [&](int m, int o) { u32x2 v = *(const LDSP u32x2*)(pa2 + 512 * (7 - m) + o); A[m][2] = v[0]; A[m][3] = v[1]; };
#pragma unroll 1
  for (int dl = 0; dl < 8; ++dl) {
    const int st = dl < 7 ? 64 : 0;
    const int bk = dl > 0 ? st : 0;
    pa -= st; pa2 -= st; pb -= st;
#pragma unroll
    for (int m = 0; m < 8; ++m) {
#pragma unroll
      for (int J = 7; J >= m; --J) {
        acc[J] = mfma_bf(A[m], Bf[J - m], acc[J]);
        const int k = 7 - J;
        if (k == 0) Bf[7 - m] = *(const u32x4*)(pb + 512 * (7 - m));
        else if (m >= 1 && m <= 5 && k == 1) ldA0(m - 1, 0);
        else if (m >= 1 && m <= 5 && k == 2) ldA1(m - 1, 0);
        else if (m == 6 && k == 1) ldA0(5, 0);
        SCHED();
      }
    }
    ldA1(5, 0); ldA0(6, 0); ldA1(6, 0); ldA0(7, 0); ldA1(7, 0);
    SCHED();
    (void)bk;
  }
  STAMP(P.ws, blockIdx.x, 2, 7);
  {
    const bool wd = li & 1;
    const u32 yo = WS_YT + (d * SEQ + 32 * (li >> 1) + 8 * g + (wd ? 0 : 4)) * 2;
#pragma unroll
    for (int J = 0; J < 8; ++J) {
      const u32 p0 = pk_bf(acc[J][0], acc[J][1]), p1 = pk_bf(acc[J][2], acc[J][3]);
      const u32 rcv = __builtin_amdgcn_mov_dpp(wd ? p1 : p0, 0xB1, 0xf, 0xf, true);
      st8_wt(rs, yo + 512 * J, wd ? u32x2{p0, rcv} : u32x2{rcv, p1});
    }
  }
  STAMP(P.ws, blockIdx.x, 2, 8);
}

constexpr int G_WSLOT = 20480, G_XR = 102400, G_XSLOT = 16384, G_WO = 151552, G_RED = 159744, G_GW = 161792;
constexpr int G_PS = G_XR + G_XSLOT;
#define BAR() asm volatile("s_waitcnt lgkmcnt(0)\n\ts_barrier" ::: "memory")

DI void role_gemm(const P1& P, int hg, int p, int s0, char* smem) {
  const int tid = threadIdx.x, lane = tid & 63;
  const int w = __builtin_amdgcn_readfirstlane(tid >> 6);
  const int li = lane & 15, g = lane >> 4;
  const rsrc_t rs = ws_rsrc(P.ws);
  STAMP(P.ws, blockIdx.x, 1, 1);
  const int mh = p >> 2, mcq = p & 3;
  const f32x4 zero = {0.f, 0.f, 0.f, 0.f};
  const u32x2 z2 = {0u, 0u};
  const int sw = (li >> 1) & 7;
  const char* xr = smem + G_XR + (32 * (w & 3) + li) * 128;
  const int xs0 = (g ^ sw) * 16, xs1 = ((4 + g) ^ sw) * 16;
  const int wrow = (8 * g + (li >> 2)) * 320 + (li & 3) * 8;
  const char* wr_e = smem + wrow + (g & 1) * 32;
  const char* wr_o = smem + wrow - (g & 1) * 32;
  auto wfrag = [&](int un, int ks, int t) {
    const char* a = ((t & 1) ? wr_o : wr_e) + (un % 5) * G_WSLOT + ks * 10240 + t * 32;
    return cat(tr_read(a), tr_read(a + 1280));
  };
  u32x2* red = (u32x2*)(smem + G_RED);

  if (w >= 4) {
    const int qw = w - 4;
    const u32 lds0 = (u32)(size_t)(LDSP char*)smem;
    auto stage_of = [&](int j) { int s = s0 + j; return s >= 10 ? s - 10 : s; };
    u32 xo[4], wc[5];
    u32 hmask = 0;
#pragma unroll
    for (int i = 0; i < 4; ++i) {
      int r = 32 * qw + 8 * i + (lane >> 3);
      int c = (lane & 7) ^ ((r >> 1) & 7);
      xo[i] = ((128 * p + r) * DM + 8 * c) * 2;
    }
    auto issue_x = [&](int j) {
      const char* sb = (const char*)P.u + stage_of(j) * 128;
      const u32 db = lds0 + G_XR + (j % 3) * G_XSLOT + qw * 4096;
#pragma unroll
      for (int i = 0; i < 4; ++i) glds1(sb, db + i * 1024, xo[i]);
    };
    issue_x(0);
#pragma unroll
    for (int i = 0; i < 5; ++i) {
      int id = 64 * (5 * qw + i) + lane;
      int r = id / 20, c = (id - 20 * r) ^ (((r >> 3) & 1) << 1);
      int h = c >= 10;
      wc[i] = (r * NW + 80 * hg + 8 * (c - 10 * h)) * 2;
      hmask |= h << i;
    }
    auto issue_w = [&](int un) {
      const u32 db = lds0 + (un % 5) * G_WSLOT + qw * 5120;
      const int sa = stage_of(un < 10 ? un : 2 * (un - 10));
      const int dlt = un < 10 ? 1280 : (stage_of(2 * (un - 10) + 1) - sa) * (64 * NW * 2);
      const int neg = dlt < 0 ? dlt : 0;
      const u32 dlo = (u32)(-neg), dhi = (u32)(dlt - neg);
      const char* sb = (const char*)P.wqkv + (size_t)sa * (64 * NW * 2) + (un < 10 ? 1280 : 0) + neg;
#pragma unroll
      for (int i = 0; i < 5; ++i) glds1(sb, db + i * 1024, wc[i] + (((hmask >> i) & 1) ? dhi : dlo));
    };
    glds1((const char*)P.gw, lds0 + G_GW + (qw & 1) * 1024, min((qw & 1) * 1024 + lane * 16, 1264));
    issue_w(0); issue_x(1); issue_w(1); issue_w(2); issue_w(3);
    const bool gate = hg == (p & 7);
    f32x4 ga[2] = {zero, zero};
    u32x4 keep[20][2];
    f32x4 aq[2][5];
#pragma unroll
    for (int c = 0; c < 5; ++c) { aq[0][c] = zero; aq[1][c] = zero; }
    auto qunit = [&](int j) {
      u32x4 bq[5];
#pragma unroll
      for (int n = 0; n < 5; ++n) bq[n] = wfrag(10 + j, 0, n);
#pragma unroll
      for (int n = 0; n < 20; ++n) {
        const int ks = n / 10, t = n % 10;
        const u32x4 bb = bq[n % 5];
        aq[0][t % 5] = mfma_bf(bb, keep[4 * j + (t < 5 ? 0 : 2) + ks][0], aq[0][t % 5]);
        aq[1][t % 5] = mfma_bf(bb, keep[4 * j + (t < 5 ? 0 : 2) + ks][1], aq[1][t % 5]);
        if (n + 5 < 20) bq[n % 5] = wfrag(10 + j, (n + 5) / 10, (n + 5) % 10);
        SCHED();
      }
    };
    auto lstep = [&](auto UN) {
      constexpr int un = decltype(UN)::value;
      if (un == 0) wait_vm<19>();
      else if (un == 1) wait_vm<19>();
      else if (un == 3) wait_vm<16>();
      else if (un < 9) wait_vm<14>();
      else wait_vm<5>();
      BAR();
#pragma unroll
      for (int r = 0; r < 2; ++r) {
        keep[2 * un][r] = *(const u32x4*)(xr + (un % 3) * G_XSLOT + r * 2048 + xs0);
        keep[2 * un + 1][r] = *(const u32x4*)(xr + (un % 3) * G_XSLOT + r * 2048 + xs1);
      }
      if (un + 2 < 10) issue_x(un + 2);
      issue_w(un + 4);
      if (gate) {
        const char* gp = smem + G_GW + stage_of(un) * 128 + g * 16;
        const u32x4 g0 = *(const u32x4*)gp, g1 = *(const u32x4*)(gp + 64);
#pragma unroll
        for (int r = 0; r < 2; ++r) { ga[r] = mfma_bf(keep[2 * un][r], g0, ga[r]); ga[r] = mfma_bf(keep[2 * un + 1][r], g1, ga[r]); }
      }
      if (un == 2) {
        const char* wob = (const char*)P.wo + (size_t)(80 * hg + 20 * mh) * (DM * 2) + mcq * 320;
#pragma unroll
        for (int i = 0; i < 2; ++i) {
          int id = min(64 * (2 * qw + i) + lane, 399);
          int r = id / 20, c = id - 20 * r;
          glds1(wob, lds0 + G_WO + (2 * qw + i) * 1024, r * (DM * 2) + c * 16);
        }
      }
    };
    lstep(IC<0>{}); lstep(IC<1>{}); lstep(IC<2>{}); lstep(IC<3>{}); lstep(IC<4>{});
    lstep(IC<5>{}); lstep(IC<6>{}); lstep(IC<7>{}); lstep(IC<8>{}); lstep(IC<9>{});
    qunit(0); qunit(1);
    wait_vm<0>(); BAR();
    issue_w(14);
    qunit(2);
    wait_vm<0>(); BAR();
    qunit(3); qunit(4);
    u32x2 pq[2][5];
#pragma unroll
    for (int r = 0; r < 2; ++r)
#pragma unroll
      for (int c = 0; c < 5; ++c) pq[r][c] = pk4_bf(phi4(aq[r][c]));
#pragma unroll
    for (int r = 0; r < 2; ++r)
      asm volatile("" : "+v"(pq[r][0]), "+v"(pq[r][1]), "+v"(pq[r][2]), "+v"(pq[r][3]), "+v"(pq[r][4]));
    if (gate) {
      const f32x4 gs = (li & 1) ? ga[1] : ga[0];
      f32x4 go;
#pragma unroll
      for (int r = 0; r < 4; ++r) go[r] = __builtin_amdgcn_rcpf(1.f + __builtin_amdgcn_exp2f(-1.44269504089f * gs[r]));
      if (li < 2) st16_wt(rs, WS_GT + (128 * p + 32 * qw + 16 * li + 4 * g) * 4, __builtin_bit_cast(u32x4, go));
    }
    BAR();
    const int hs = li & 3;
    u32x2 zf[5];
#pragma unroll
    for (int c = 0; c < 5; ++c) {
      const int hc = g < c ? c - 1 : c;
      zf[c] = hs == hc ? red[100 + c * 4 + g] : z2;
    }
#pragma unroll
    for (int r = 0; r < 2; ++r) {
      f32x4 den = mfma_bf(cat(zf[0], zf[1]), cat(pq[r][0], pq[r][1]), zero);
      den = mfma_bf(cat(zf[2], zf[3]), cat(pq[r][2], pq[r][3]), den);
      den = mfma_bf(cat(zf[4], z2), cat(pq[r][4], z2), den);
      float rc[4];
#pragma unroll
      for (int j = 0; j < 4; ++j) rc[j] = __builtin_amdgcn_rcpf(den[j] + 1e-6f);
      u32x2 o[5];
#pragma unroll
      for (int c = 0; c < 5; ++c) {
        const float s = c == 0 ? rc[0] : (c == 4 ? rc[3] : (g < c ? rc[c - 1] : rc[c]));
        o[c] = u32x2{pk_bf(bf_lo(pq[r][c][0]) * s, bf_hi(pq[r][c][0]) * s), pk_bf(bf_lo(pq[r][c][1]) * s, bf_hi(pq[r][c][1]) * s)};
      }
      const u32 aqp = WS_AQ + ((8 * p + 2 * qw + r) * 20) * 1024 + lane * 16;
      st16_wt(rs, aqp + (2 * hg) * 1024, cat(o[0], o[1]));
      st16_wt(rs, aqp + (2 * hg + 1) * 1024, cat(o[2], o[3]));
      st8_wt(rs, aqp + (16 + (hg >> 1)) * 1024 + (hg & 1) * 8, o[4]);
    }
    return;
  }

  __builtin_amdgcn_s_setprio(2);
  const u64 tag = next_tag(P.ws + WS_EP + hg * 64);
  const Tag tg = mk_tag(tag);
  f32x4 acc[2][10];
#pragma unroll
  for (int c = 0; c < 10; ++c) { acc[0][c] = zero; acc[1][c] = zero; }
  u32x4 b0[10], b1[10], xa[2], xb[2];
  auto kstep = [&](auto UN) {
    constexpr int un = decltype(UN)::value;
    BAR();
    xa[0] = *(const u32x4*)(xr + (un % 3) * G_XSLOT + xs0);
    xa[1] = *(const u32x4*)(xr + (un % 3) * G_XSLOT + 2048 + xs0);
#pragma unroll
    for (int t = 0; t < 10; ++t) {
      b0[t] = wfrag(un, 0, t);
      if (un > 0) { acc[0][t] = mfma_bf(xb[0], b1[t], acc[0][t]); acc[1][t] = mfma_bf(xb[1], b1[t], acc[1][t]); }
      SCHED();
    }
    xb[0] = *(const u32x4*)(xr + (un % 3) * G_XSLOT + xs1);
    xb[1] = *(const u32x4*)(xr + (un % 3) * G_XSLOT + 2048 + xs1);
#pragma unroll
    for (int t = 0; t < 10; ++t) {
      b1[t] = wfrag(un, 1, t);
      acc[0][t] = mfma_bf(xa[0], b0[t], acc[0][t]); acc[1][t] = mfma_bf(xa[1], b0[t], acc[1][t]);
      SCHED();
    }
  };
  kstep(IC<0>{}); kstep(IC<1>{}); kstep(IC<2>{}); kstep(IC<3>{}); kstep(IC<4>{});
  kstep(IC<5>{}); kstep(IC<6>{}); kstep(IC<7>{}); kstep(IC<8>{}); kstep(IC<9>{});
#pragma unroll
  for (int t = 0; t < 10; ++t) { acc[0][t] = mfma_bf(xb[0], b1[t], acc[0][t]); acc[1][t] = mfma_bf(xb[1], b1[t], acc[1][t]); }
  STAMP(P.ws, blockIdx.x, 1, 2);
  {
    u32x4 fr[10];
#pragma unroll
    for (int t = 0; t < 10; ++t)
      fr[t] = t < 5 ? cat(pk4_bf(phi4(acc[0][t])), pk4_bf(phi4(acc[1][t]))) : cat(pk4_bf(acc[0][t]), pk4_bf(acc[1][t]));
    const u32x4 ones = {0x3f803f80u, 0x3f803f80u, 0x3f803f80u, 0x3f803f80u};
    f32x4* ps = (f32x4*)(smem + G_PS) + w * 448;
    f32x4 zsel = zero;
#pragma unroll
    for (int c = 0; c < 5; ++c) {
      const f32x4 dd = mfma_bf(fr[5 + c], fr[c], zero);
      f32x4 dm = zero, dp = zero;
      if (c > 0) dm = mfma_bf(fr[4 + c], fr[c], zero);
      if (c < 4) dp = mfma_bf(fr[6 + c], fr[c], zero);
      const f32x4 dz = mfma_bf(fr[c], ones, zero);
      const bool um = li < 4 * c;
      const int jd = g - c + (um ? 5 : 0), jo = jd + (um ? -4 : 4);
      const int eb = (16 * c + li) * 5;
      ps[(jd >= 0 && jd < 5) ? eb + jd : 420 + li] = dd;
      ps[(jo >= 0 && jo < 5) ? eb + jo : 420 + li] = c == 0 ? dp : (c == 4 ? dm : (um ? dm : dp));
      if (c == 0 || li == c) zsel = dz;
    }
    ps[li < 5 ? 400 + 4 * li + g : 420 + li] = zsel;
  }
  BAR();
  {
    const f32x4* ps = (const f32x4*)(smem + G_PS);
#pragma unroll
    for (int k = 0; k < 2; ++k) {
      const int e = tid + 256 * k;
      if (e < 420) {
        const f32x4 v = (ps[e] + ps[448 + e]) + (ps[896 + e] + ps[1344 + e]);
        const u32 o = e < 400 ? WS_KV + (hg * 400 + e) * 256 : WS_ZP + (hg * 20 + e - 400) * 256;
        st16_dev(rs, o + p * 16, tag4(v, tg));
      }
    }
  }
  STAMP(P.ws, blockIdx.x, 1, 3);
  u32x4 wf[3];
  {
    const char* lo = smem + G_WO + (4 * g + (li >> 2)) * 320 + (li & 3) * 8;
    const char* hi = smem + G_WO + (16 + (li >> 2)) * 320 + (li & 3) * 8;
#pragma unroll
    for (int k = 0; k < 3; ++k) {
      const int t = min(w + 4 * k, 9);
      wf[k] = cat(tr_read(lo + t * 32), tr_read(hi + t * 32));
    }
  }
  BAR();
  {
    const int he = min(tid >> 1, 99), ze = min(tid >> 3, 19);
    const u32 h1o = WS_KV + ((hg * 4 + mh) * 100 + he) * 256 + (tid & 1) * 128;
    const u32 zo = WS_ZP + (hg * 20 + ze) * 256 + (tid & 7) * 32;
    u32x4 h1[8], zv[2];
    for (u32 spin = 0; spin < SPIN_MAX; ++spin) {
#pragma unroll
      for (int i = 0; i < 8; ++i) h1[i] = ld16_dev(rs, h1o + 16 * i);
      zv[0] = ld16_dev(rs, zo); zv[1] = ld16_dev(rs, zo + 16);
      u32 bad = tag_bad(zv[0], tg) | tag_bad(zv[1], tg);
#pragma unroll
      for (int i = 0; i < 8; ++i) bad |= tag_bad(h1[i], tg);
      if (__all((bad & 0xfff) == 0)) break;
      asm volatile("" ::: "memory");
    }
    f32x4 ks = ((untag(h1[0]) + untag(h1[1])) + (untag(h1[2]) + untag(h1[3]))) +
               ((untag(h1[4]) + untag(h1[5])) + (untag(h1[6]) + untag(h1[7])));
    f32x4 zs = untag(zv[0]) + untag(zv[1]);
#pragma unroll
    for (int r = 0; r < 4; ++r) {
      ks[r] = dpp_add<0xB1>(ks[r]);
      zs[r] = dpp_add<0x141>(dpp_add<0x4E>(dpp_add<0xB1>(zs[r])));
    }
    if ((tid & 1) == 0 && tid < 200) red[he] = pk4_bf(ks);
    if ((tid & 7) == 0 && tid < 160) red[100 + ze] = pk4_bf(zs);
  }
  BAR();
  STAMP(P.ws, blockIdx.x, 1, 5);
  if (p == 15 && tid == 0) st8_dev(P.ws + WS_EP + hg * 64, tag);
  {
    const int ib = 16 + (li & 3);
    const u32x4 a0 = cat(red[li * 5 + g], g == 0 ? red[li * 5 + 4] : z2);
    const u32x4 a1 = cat(red[ib * 5 + g], g == 0 ? red[ib * 5 + 4] : z2);
    auto dst = [&](int qi) {
      const int mc = qi >> 2;
      const int ksl = mc < 4 ? 2 * hg + (mc >> 1) : 16 + (hg >> 1), hf = mc < 4 ? (mc & 1) : (hg & 1);
      return (u32)(ksl * 1024 + (li + 16 * (qi & 3)) * 16 + hf * 8);
    };
    const u32 cb = WS_MW + (10 * mcq + w) * 20480;
    const u32 d0 = dst(5 * mh + g), d1 = dst(5 * mh + 4);
    u32x2 mb = z2;
#pragma unroll
    for (int k = 0; k < 3; ++k) {
      const u32x2 m0 = pk4_bf(mfma_bf(a0, wf[k], zero)), m1 = pk4_bf(mfma_bf(a1, wf[k], zero));
      if (k < 2 || w < 2) st8_wt(rs, cb + k * (4 * 20480) + d0, m0);
      if (g == k) mb = m1;
    }
    if (g < 2 || (g == 2 && w < 2)) st8_wt(rs, cb + g * (4 * 20480) + d1, mb);
  }
  STAMP(P.ws, blockIdx.x, 1, 6);
}

__global__ __launch_bounds__(512, 2) void k1(P1 P) {
  extern __shared__ __attribute__((aligned(16))) char smem[];
  const int b = blockIdx.x, x = b & 7, r = b >> 3;
  if (r < 16) {
    const int hl = r & 3, pl = r >> 2;
    role_gemm(P, (x & 1) * 4 + hl, (x >> 1) * 4 + pl, ((5 * (pl + hl)) / 2 + (hl & 1)) % 10, smem);
  } else {
    role_conv(P, x * 10 + (r - 16), smem);
  }
}

__global__ __launch_bounds__(256, 2) void k2(const char* ws, u16* out) {
  extern __shared__ __attribute__((aligned(16))) char smem[];
  const int tid = threadIdx.x, lane = tid & 63, li = lane & 15, g = lane >> 4;
  const int w = __builtin_amdgcn_readfirstlane(tid >> 6);
  const int b = blockIdx.x, x = b & 7, r_ = b >> 3;
  const int rl = r_ >> 2, cl = r_ & 3;
  const int rg = (x >> 1) * 8 + rl, cg = (x & 1) * 4 + cl;
  STAMP(ws, 1024 + blockIdx.x, 5, 1);
  const int s0 = (rl * 5 / 2 + cl * 5) % 20;
  const int rt = rg * 4 + w;
  const int t0 = rt * 16 + 4 * g;
  u32x2 ya[5];
#pragma unroll
  for (int c = 0; c < 5; ++c) ya[c] = *(const u32x2*)((const u16*)(ws + WS_YT) + (size_t)(cg * 80 + c * 16 + li) * SEQ + t0);
  const f32x4 gt = *(const f32x4*)((const float*)(ws + WS_GT) + t0);
  const char* ab = ws + WS_AQ + (size_t)rt * 20480;
  const u32 lds0 = (u32)(size_t)(LDSP char*)smem;
  const char* mb = ws + WS_MW + (size_t)cg * 5 * 20480;
  const u32 lo16 = lane * 16;
  u32x4 a[20];
  auto issue = [&](int q, int i) {
    if (i < 5) {
      int ks = 4 * q + w + s0; ks = ks >= 20 ? ks - 20 : ks;
      glds1(mb + ks * 1024 + i * 20480, lds0 + (4 * q + w) * 5120 + i * 1024, lo16);
    } else {
      int ks = 4 * q + (i - 5) + s0; ks = ks >= 20 ? ks - 20 : ks;
      asm volatile("global_load_dwordx4 %0, %1, %2" : "=v"(a[4 * q + i - 5]) : "v"(lo16), "s"(ab + ks * 1024) : "memory");
    }
  };
#pragma unroll
  for (int q = 0; q < 3; ++q)
#pragma unroll
    for (int i = 0; i < 9; ++i) issue(q, i);
  f32x4 acc[5];
#pragma unroll
  for (int c = 0; c < 5; ++c) acc[c] = f32x4{0.f, 0.f, 0.f, 0.f};
  const char* mrd = smem + lane * 16;
#pragma unroll
  for (int q = 0; q < 5; ++q) {
    if (q < 3) wait_vm_bar<18>(); else if (q == 3) wait_vm_bar<9>(); else wait_vm_bar<0>();
    asm volatile("" : "+v"(a[4 * q]), "+v"(a[4 * q + 1]), "+v"(a[4 * q + 2]), "+v"(a[4 * q + 3]));
    u32x4 bf[20];
#pragma unroll
    for (int i = 0; i < 20; ++i) bf[i] = *(const u32x4*)(mrd + (q * 20 + i) * 1024);
    SCHED();
#pragma unroll
    for (int i = 0; i < 20; ++i) {
      acc[i % 5] = mfma_bf(a[4 * q + i / 5], bf[i], acc[i % 5]);
      if (q + 3 < 5 && (i & 1) && i < 18) { issue(q + 3, i >> 1); SCHED(); }
    }
  }
  STAMP(ws, 1024 + blockIdx.x, 5, 3);
  const bool odd = li & 1;
  u16* op = out + (size_t)(t0 + (odd ? 2 : 0)) * DM + cg * 80 + (li & ~1);
#pragma unroll
  for (int c = 0; c < 5; ++c) {
    float v0 = gt[0] * bf_lo(ya[c][0]) + (1.f - gt[0]) * acc[c][0];
    float v1 = gt[1] * bf_hi(ya[c][0]) + (1.f - gt[1]) * acc[c][1];
    float v2 = gt[2] * bf_lo(ya[c][1]) + (1.f - gt[2]) * acc[c][2];
    float v3 = gt[3] * bf_hi(ya[c][1]) + (1.f - gt[3]) * acc[c][3];
    u32 p01 = pk_bf(v0, v1), p23 = pk_bf(v2, v3);
    u32 send = odd ? p01 : p23, keep = odd ? p23 : p01;
    u32 recv = __builtin_amdgcn_mov_dpp(send, 0xB1, 0xf, 0xf, true);
    u32 lo = odd ? recv : keep, hi = odd ? keep : recv;
    *(u32*)(op + c * 16) = __builtin_amdgcn_perm(hi, lo, 0x05040100u);
    *(u32*)(op + c * 16 + DM) = __builtin_amdgcn_perm(hi, lo, 0x07060302u);
  }
  STAMP(ws, 1024 + blockIdx.x, 5, 4);
}

extern "C" void kernel_launch(void* const* d_in, const int* in_sizes, int n_in, void* d_out, int out_size,
                              void* d_ws, size_t ws_size, hipStream_t stream) {
  P1 P;
  P.u = (const u16*)d_in[0]; P.are = (const u16*)d_in[1]; P.aim = (const u16*)d_in[2];
  P.bre = (const u16*)d_in[3]; P.bim = (const u16*)d_in[4]; P.cre = (const u16*)d_in[5]; P.cim = (const u16*)d_in[6];
  P.logdt = (const u16*)d_in[7]; P.dskip = (const u16*)d_in[8]; P.wqkv = (const u16*)d_in[10];
  P.wo = (const u16*)d_in[11]; P.gw = (const u16*)d_in[12]; P.ws = (char*)d_ws;
  (void)hipFuncSetAttribute((const void*)k1, hipFuncAttributeMaxDynamicSharedMemorySize, 163840);
  (void)hipFuncSetAttribute((const void*)k2, hipFuncAttributeMaxDynamicSharedMemorySize, 102400);
  k1<<<208, 512, 163840, stream>>>(P);
  k2<<<256, 256, 102400, stream>>>((const char*)d_ws, (u16*)d_out);
}
```

```cpp
#define NQE 4
#define CODE_BYTES 50000
#include <hip/hip_runtime.h>
#include <stdint.h>

typedef unsigned short u16;
typedef unsigned int u32;
typedef unsigned long long u64;
typedef __attribute__((ext_vector_type(8))) __bf16 bf16x8;
typedef __attribute__((ext_vector_type(2))) __bf16 bf16x2;
typedef __attribute__((ext_vector_type(8))) _Float16 f16x8;
typedef __attribute__((ext_vector_type(2))) _Float16 f16x2;
typedef __attribute__((ext_vector_type(4))) short s16x4;
typedef __attribute__((ext_vector_type(4))) float f32x4;
typedef __attribute__((ext_vector_type(2))) float f32x2;
typedef __attribute__((ext_vector_type(4))) u32 u32x4;
typedef __attribute__((ext_vector_type(3))) u32 u32x3;
typedef __attribute__((ext_vector_type(2))) u32 u32x2;
#define LDSP __attribute__((address_space(3)))
#define GLBP __attribute__((address_space(1)))
#define DI __device__ __forceinline__

constexpr int SEQ = 2048, DM = 640, NW = 1920;

constexpr u32 WS_AQ = 0;
constexpr u32 WS_MW = WS_AQ + 128u * 20 * 1024;
constexpr u32 WS_YT = WS_MW + 40u * 20 * 1024;
constexpr u32 WS_GT = WS_YT + 640u * 2048 * 2;
constexpr u32 WS_KV = WS_GT + 2048u * 4;
constexpr u32 KV_STR = 424 * 16;
constexpr u32 WS_EP = WS_KV + 128u * KV_STR;
constexpr u32 WS_END = WS_EP + 8u * 64;
constexpr u64 EPOCH_STEP = 0x9E37001001001001ull;
constexpr u32 SPIN_MAX = 200000;

constexpr size_t WS_DBG = 64u << 20;
#define STAMP(ws, slot, role, i) do {} while (0)
#define STAMPT(ws, t, i) do {} while (0)

DI float bf_lo(u32 v) { return __uint_as_float(v << 16); }
DI float bf_hi(u32 v) { return __uint_as_float(v & 0xffff0000u); }
DI float bf1(u16 v) { return __uint_as_float(((u32)v) << 16); }
DI u32 pk_bf(float a, float b) { f32x2 v = {a, b}; return __builtin_bit_cast(u32, __builtin_convertvector(v, bf16x2)); }
DI u32 pk_h(float a, float b) { f32x2 v = {a, b}; return __builtin_bit_cast(u32, __builtin_convertvector(v, f16x2)); }
DI u32x2 pk4_bf(f32x4 v) { return u32x2{pk_bf(v[0], v[1]), pk_bf(v[2], v[3])}; }
DI bf16x8 as_bf8(u32x4 v) { return __builtin_bit_cast(bf16x8, v); }
DI f16x8 as_h8(u32x4 v) { return __builtin_bit_cast(f16x8, v); }
DI u32x4 cat(u32x2 a, u32x2 b) { return u32x4{a[0], a[1], b[0], b[1]}; }
DI f32x4 mfma_bf(u32x4 a, u32x4 b, f32x4 c) { return __builtin_amdgcn_mfma_f32_16x16x32_bf16(as_bf8(a), as_bf8(b), c, 0, 0, 0); }
DI void mfma_ip(f32x4& c, u32x4 a, u32x4 b) { asm volatile("v_mfma_f32_16x16x32_bf16 %0, %1, %2, %0" : "+v"(c) : "v"(a), "v"(b)); }
DI float vmax(float a, float b) { return __builtin_amdgcn_fmed3f(a, b, __builtin_inff()); }
DI f32x4 mfma_h(u32x4 a, u32x4 b, f32x4 c) { return __builtin_amdgcn_mfma_f32_16x16x32_f16(as_h8(a), as_h8(b), c, 0, 0, 0); }
DI float phi(float t) { return (t > 0.f ? t + 1.f : __builtin_amdgcn_exp2f(t * 1.44269504089f)) + 1e-6f; }
DI float exp2_clamp(float x) { float d; asm("v_exp_f32 %0, %1 clamp" : "=v"(d) : "v"(x)); return d; }
DI f32x4 phi4(f32x4 v) {
  const f32x4 s = v * 1.44269504089f;
  const f32x4 e = {exp2_clamp(s[0]), exp2_clamp(s[1]), exp2_clamp(s[2]), exp2_clamp(s[3])};
  const f32x4 m = {vmax(v[0], 1e-6f), vmax(v[1], 1e-6f), vmax(v[2], 1e-6f), vmax(v[3], 1e-6f)};
  return m + e;
}
DI u32x2 tr_read(const char* l) {
  s16x4 t = __builtin_amdgcn_ds_read_tr16_b64_v4i16((LDSP s16x4*)l);
  return __builtin_bit_cast(u32x2, t);
}
template <int CTRL> DI float dpp_add(float v) {
  return v + __int_as_float(__builtin_amdgcn_mov_dpp(__float_as_int(v), CTRL, 0xf, 0xf, true));
}
template <int N> struct IC { static constexpr int value = N; };
#define BAR() asm volatile("s_waitcnt lgkmcnt(0)\n\ts_barrier" ::: "memory")
#define SCHED() __builtin_amdgcn_sched_barrier(0)
#define WAIT_LGKM() asm volatile("s_waitcnt lgkmcnt(0)" ::: "memory")
template <int N> DI void wait_vm() { asm volatile("s_waitcnt vmcnt(%0)" ::"n"(N) : "memory"); }
template <int N> DI void wait_vm_bar() { asm volatile("s_waitcnt vmcnt(%0) lgkmcnt(0)\n\ts_barrier" ::"n"(N) : "memory"); }

DI void glds1(const char* sbase, u32 dst, u32 off) {
  u32 keep;
  asm volatile("s_mov_b32 %0, m0\n\ts_mov_b32 m0, %2\n\ts_nop 0\n\t"
               "global_load_lds_dwordx4 %3, %1\n\t"
               "s_mov_b32 m0, %0"
               : "=&s"(keep) : "s"(sbase), "s"(dst), "v"(off) : "memory");
}
typedef __amdgpu_buffer_rsrc_t rsrc_t;
DI rsrc_t ws_rsrc(char* ws) { return __builtin_amdgcn_make_buffer_rsrc((void*)ws, (short)0, (int)WS_END, 0x00020000); }
DI void st16_dev(rsrc_t r, u32 off, u32x4 v) { __builtin_amdgcn_raw_buffer_store_b128(v, r, off, 0, 16); }
#define WT_AUX 16
DI void st16_wt(rsrc_t r, u32 off, u32x4 v) { __builtin_amdgcn_raw_buffer_store_b128(v, r, off, 0, WT_AUX); }
DI void st8_wt(rsrc_t r, u32 off, u32x2 v) { __builtin_amdgcn_raw_buffer_store_b64(v, r, off, 0, WT_AUX); }
DI void st4_dev(rsrc_t r, u32 off, u32 v) { __builtin_amdgcn_raw_buffer_store_b32(v, r, off, 0, 16); }
DI u32x4 ld16_dev(rsrc_t r, u32 off) { return __builtin_amdgcn_raw_buffer_load_b128(r, off, 0, 16); }
DI void st8_dev(char* p, u64 v) { __hip_atomic_store((GLBP u64*)p, v, __ATOMIC_RELAXED, __HIP_MEMORY_SCOPE_AGENT); }
DI u64 ld8_dev(const char* p) { return __hip_atomic_load((GLBP u64*)p, __ATOMIC_RELAXED, __HIP_MEMORY_SCOPE_AGENT); }
struct Tag { u32 t0, t1, t2, t3; };
DI Tag mk_tag(u64 t) { Tag g; g.t0 = (u32)t & 0xfff; g.t1 = (u32)(t >> 12) & 0xfff; g.t2 = (u32)(t >> 24) & 0xfff; g.t3 = (u32)(t >> 36) & 0xfff; return g; }
DI u32x4 tag4(f32x4 v, Tag t) {
  u32x4 b = __builtin_bit_cast(u32x4, v);
  return u32x4{((b[0] + 0x800u) & 0xfffff000u) | t.t0, ((b[1] + 0x800u) & 0xfffff000u) | t.t1,
               ((b[2] + 0x800u) & 0xfffff000u) | t.t2, ((b[3] + 0x800u) & 0xfffff000u) | t.t3};
}
DI u32 tag_bad(u32x4 b, Tag t) { return (b[0] ^ t.t0) | (b[1] ^ t.t1) | (b[2] ^ t.t2) | (b[3] ^ t.t3); }
DI f32x4 untag(u32x4 b) { return __builtin_bit_cast(f32x4, b & 0xfffff000u); }

DI u64 next_tag(const char* p) {
  u64 e;
  asm volatile("s_load_dwordx2 %0, %1, 0x0\n\ts_waitcnt lgkmcnt(0)" : "=s"(e) : "s"(p) : "memory");
  return e + EPOCH_STEP;
}

struct P1 {
  const u16 *u, *are, *aim, *bre, *bim, *cre, *cim, *logdt, *dskip, *wqkv, *wo, *gw;
  char* ws;
};

constexpr int C_UR0 = 256, C_UR1 = 4480, C_KN0 = 8704, C_KN1 = 13344, C_CH = 18176, C_SSTR = 544;

struct cpx { float r0, r1, i0, i1; };
DI float vmul(float a, float b) { float d; asm("v_mul_f32 %0, %1, %2" : "=v"(d) : "v"(a), "v"(b)); return d; }
DI float vfma(float a, float b, float c) { float d; asm("v_fma_f32 %0, %1, %2, %3" : "=v"(d) : "v"(a), "v"(b), "v"(c)); return d; }
DI float vfms(float a, float b, float c) { float d; asm("v_fma_f32 %0, %1, %2, -%3" : "=v"(d) : "v"(a), "v"(b), "v"(c)); return d; }
DI cpx cmul(cpx a, cpx b) {
  cpx o;
  f32x2 ar = {a.r0, a.r1}, ai = {a.i0, a.i1}, br = {b.r0, b.r1}, bi = {b.i0, b.i1};
  f32x2 r = ar * br - ai * bi, i = ar * bi + ai * br;
  o.r0 = r[0]; o.r1 = r[1]; o.i0 = i[0]; o.i1 = i[1];
  return o;
  o.r0 = vfms(a.r0, b.r0, vmul(a.i0, b.i0)); o.i0 = vfma(a.r0, b.i0, vmul(a.i0, b.r0));
  o.r1 = vfms(a.r1, b.r1, vmul(a.i1, b.i1)); o.i1 = vfma(a.r1, b.i1, vmul(a.i1, b.r1));
  return o;
}
DI cpx cexp_l(f32x2 er, f32x2 th, float l) {
  cpx o;
  float m0 = __builtin_amdgcn_exp2f(er[0] * l), m1 = __builtin_amdgcn_exp2f(er[1] * l);
  float p0 = __builtin_amdgcn_fractf(th[0] * l), p1 = __builtin_amdgcn_fractf(th[1] * l);
  o.r0 = m0 * __builtin_amdgcn_cosf(p0); o.r1 = m1 * __builtin_amdgcn_cosf(p1);
  o.i0 = m0 * __builtin_amdgcn_sinf(p0); o.i1 = m1 * __builtin_amdgcn_sinf(p1);
  return o;
}
DI void put_row(char* srow, cpx v) {
  u32x2 w = {pk_h(v.r0, v.i0), pk_h(v.r1, v.i1)};
  *(u32x2*)srow = w;
}
DI f32x4 kgen_tile(const char* sread, const u32x4 (&xf)[8]) {
  f32x4 a0 = {0.f, 0.f, 0.f, 0.f}, a1 = a0;
#pragma unroll
  for (int s = 0; s < 8; s += 2) {
    a0 = mfma_h(*(const u32x4*)(sread + 64 * s), xf[s], a0);
    a1 = mfma_h(*(const u32x4*)(sread + 64 * s + 64), xf[s + 1], a1);
  }
  return a0 + a1;
}

DI void role_conv(const P1& P, int cb, char* smem) {
  const int tid = threadIdx.x, lane = tid & 63;
  const int w = __builtin_amdgcn_readfirstlane(tid >> 6);
  const int d0 = cb * 8, d = d0 + w;
  char* ch = smem + w * C_CH;
  const int li = lane & 15, g = lane >> 4;
  STAMP(P.ws, blockIdx.x, 2, 1);
  const int pi = d * 128 + 2 * lane;
  u32 w_are = *(const u32*)(P.are + pi), w_aim = *(const u32*)(P.aim + pi);
  u32 w_bre = *(const u32*)(P.bre + pi), w_bim = *(const u32*)(P.bim + pi);
  u32 w_cre = *(const u32*)(P.cre + pi), w_cim = *(const u32*)(P.cim + pi);
  const u16 w_ldt = P.logdt[d], w_dsk = P.dskip[d];
  SCHED();
  const bool stager = w >= 4;
  const int sj = tid & 255;
  u32x4 ur[9];
  if (stager) {
    const u16* up = P.u + (size_t)(sj * 8) * DM + d0;
#pragma unroll
    for (int r = 0; r < 8; ++r) ur[r + 1] = *(const u32x4*)(up + r * DM);
    ur[0] = *(const u32x4*)(sj ? up - DM : up);
  }
  SCHED();
  float dt = expf(bf1(w_ldt));
  const float dsk = bf1(w_dsk);
  dt = bf_lo(pk_bf(dt, dt));
  f32x2 er = {bf_lo(w_are), bf_hi(w_are)}, th = {bf_lo(w_aim), bf_hi(w_aim)};
  er = (er * dt) * 1.44269504089f;
  th = (th * dt) * 0.15915494309f;
  cpx B, C;
  B.r0 = bf_lo(w_bre) * dt; B.r1 = bf_hi(w_bre) * dt; B.i0 = bf_lo(w_bim) * dt; B.i1 = bf_hi(w_bim) * dt;
  C.r0 = bf_lo(w_cre); C.r1 = bf_hi(w_cre); C.i0 = bf_lo(w_cim); C.i1 = bf_hi(w_cim);
  cpx c = cmul(C, B);
  float mx = fmaxf(fmaxf(fabsf(c.r0), fabsf(c.r1)), fmaxf(fabsf(c.i0), fabsf(c.i1)));
  u32 me = __float_as_uint(mx) & 0x7f800000u;
  me = max(me, (u32)__builtin_amdgcn_mov_dpp((int)me, 0xB1, 0xf, 0xf, true));
  me = max(me, (u32)__builtin_amdgcn_mov_dpp((int)me, 0x4E, 0xf, 0xf, true));
  me = max(me, (u32)__builtin_amdgcn_mov_dpp((int)me, 0x141, 0xf, 0xf, true));
  me = max(me, (u32)__builtin_amdgcn_mov_dpp((int)me, 0x140, 0xf, 0xf, true));
  const u32 mall = max(max((u32)__builtin_amdgcn_readlane((int)me, 0), (u32)__builtin_amdgcn_readlane((int)me, 16)),
                       max((u32)__builtin_amdgcn_readlane((int)me, 32), (u32)__builtin_amdgcn_readlane((int)me, 48)));
  const u32 mexp = min(max(mall, 0x0c000000u), 0x7c000000u);
  {
    const float inv = __uint_as_float(0x82000000u - mexp);
    c.r0 *= inv; c.r1 *= inv; c.i0 *= inv; c.i1 *= inv;
  }
  const float kscale = __uint_as_float(mexp - 0x07000000u);
  STAMP(P.ws, blockIdx.x, 2, 2);

  const cpx E1 = cexp_l(er, th, 1.f), E16 = cexp_l(er, th, 16.f), E256 = cexp_l(er, th, 256.f);
  char* sc = ch + C_KN0;
  char* swr = sc + lane * 8;
  const char* srd = sc + li * C_SSTR + g * 16;
  u32x4 xf[8];
  {
    cpx v; v.r0 = 256.f; v.r1 = 256.f; v.i0 = 0.f; v.i1 = 0.f;
    cpx e = E16; e.i0 = -e.i0; e.i1 = -e.i1;
#pragma unroll 4
    for (int o = 0; o < 16; ++o) { put_row(swr + o * C_SSTR, v); v = cmul(v, e); }
#pragma unroll
    for (int s = 0; s < 8; ++s) xf[s] = *(const u32x4*)(srd + 64 * s);
  }
  if (stager) {
    __builtin_amdgcn_s_setprio(2);
    if (sj == 0) ur[0] = u32x4{0, 0, 0, 0};
    char* ub = smem + 4080 - 16 * sj;
#pragma unroll
    for (int cc = 0; cc < 8; ++cc) {
      const u32 sel = (cc & 1) ? 0x07060302u : 0x05040100u;
      const int k = cc >> 1;
      u32x4 o1, o0;
#pragma unroll
      for (int q = 0; q < 4; ++q) {
        o1[q] = __builtin_amdgcn_perm(ur[7 - 2 * q][k], ur[8 - 2 * q][k], sel);
        o0[q] = __builtin_amdgcn_perm(ur[6 - 2 * q][k], ur[7 - 2 * q][k], sel);
      }
      *(u32x4*)(ub + cc * C_CH + C_UR1) = o1;
      *(u32x4*)(ub + cc * C_CH + C_UR0) = o0;
    }
#define BPRIO 1
    __builtin_amdgcn_s_setprio(BPRIO);
    STAMPT(P.ws, 448, 10);
    BAR();
    STAMPT(P.ws, 448, 11);
  }
  f32x4 k0, k1, k2;
  {
    cpx v = c;
#pragma unroll 4
    for (int o = 0; o < 16; ++o) { put_row(swr + o * C_SSTR, v); v = cmul(v, E1); }
    k0 = kgen_tile(srd, xf);
  }
  const cpx E2 = cmul(E1, E1), E4 = cmul(E2, E2), E8 = cmul(E4, E4);
  cpx G = cmul(c, E256);
  {
    cpx v = G;
#pragma unroll 4
    for (int o = 0; o < 8; ++o) { put_row(swr + o * C_SSTR, v); v = cmul(v, E2); }
#pragma unroll
    for (int m = 0; m < 2; ++m) {
      G = cmul(G, E256); v = G;
#pragma unroll
      for (int o = 0; o < 4; ++o) { put_row(swr + (8 + 4 * m + o) * C_SSTR, v); v = cmul(v, E4); }
    }
    k1 = kgen_tile(srd, xf);
  }
  const rsrc_t rs = ws_rsrc(P.ws);
  {
#pragma unroll
    for (int m = 0; m < 4; ++m) {
      G = cmul(G, E256);
      put_row(swr + (2 * m) * C_SSTR, G);
      put_row(swr + (2 * m + 1) * C_SSTR, cmul(G, E8));
    }
    G = cmul(G, E256);
    put_row(swr + 8 * C_SSTR, G);
    k2 = kgen_tile(srd, xf);
  }
  k0 *= kscale; k1 *= kscale; k2 *= kscale;
  STAMP(P.ws, blockIdx.x, 2, 3); STAMPT(P.ws, 448, 12);
  if (lane == 0) k0[0] += dsk;

  {
    char* kc = ch + C_KN0 + 16;
    *(u32x2*)(kc + 2 * (16 * li + 4 * g)) = pk4_bf(k0);
    *(u32x2*)(kc + 2 * (g < 2 ? 256 + 8 * li + 4 * g : 256 + 64 * g + 4 * li)) = pk4_bf(k1);
    {
      const u32x2 p2 = pk4_bf(k2);
      const int i2 = g < 2 ? 512 + 64 * g + 2 * li : (lane == 32 ? 640 : 704);
      *(u32*)(kc + 2 * i2) = p2[0];
      *(u32*)(kc + 2 * (g < 2 ? i2 + 32 : -2)) = g < 2 ? p2[1] : 0u;
    }
    const u32 s30 = *(const u32*)(kc + 1024 + 4 * lane), s31 = *(const u32*)(kc + 1028 + 4 * lane);
    const u32 s20 = *(const u32*)(kc + 768 + 4 * lane), s21 = *(const u32*)(kc + 772 + 4 * lane);
    const bool hi = lane >= 32;
    const char* q = kc + (hi ? 256 + 8 * lane : 16 * lane);
    const u32 dm = *(const u32*)(q - 4), d0 = *(const u32*)q, d1 = *(const u32*)(q + 4), d2 = *(const u32*)(q + 8);
    const u32 d3 = *(const u32*)(q + (hi ? 4 : 12));
    const u32 k510 = *(const u16*)(kc + 766);
    char* a0 = ch + C_KN0 + 512;
    char* a1 = ch + C_KN1 + 512;
    const u32 LL = 0x01000100u, HH = 0x03020302u;
    {
      const u32 a = __builtin_amdgcn_perm(s30, s30, LL), c = __builtin_amdgcn_perm(s30, s30, HH), e = __builtin_amdgcn_perm(s31, s31, LL);
      *(u32x4*)(a0 + 2048 + 32 * lane) = u32x4{a, a, s30, c};
      *(u32x4*)(a0 + 2064 + 32 * lane) = u32x4{c, c, e, e};
      *(u32x4*)(a1 + 2048 + 32 * lane) = u32x4{a, a, a, s30};
      *(u32x4*)(a1 + 2064 + 32 * lane) = u32x4{c, c, c, e};
    }
    {
      const u32 a = __builtin_amdgcn_perm(s20, s20, LL), c = __builtin_amdgcn_perm(s20, s20, HH), e = __builtin_amdgcn_perm(s21, s21, LL);
      *(u32x4*)(a0 + 1024 + 16 * lane) = u32x4{a, s20, c, e};
      *(u32x4*)(a1 + 1024 + 16 * lane) = u32x4{lane == 0 ? ((a & 0xffff0000u) | k510) : a, a, s20, c};
    }
    {
      const u32 X = 0x05040302u;
      const u32 o0 = __builtin_amdgcn_perm(d0, dm, lane > 32 ? X : 0x03020100u);
      const u32 o1 = __builtin_amdgcn_perm(d0, d0, hi ? LL : 0x03020100u);
      const u32 o2 = __builtin_amdgcn_perm(d1, d0, hi ? X : 0x07060504u);
      const u32 o3 = __builtin_amdgcn_perm(d2, d1, hi ? LL : 0x07060504u);
      const u32 o4 = __builtin_amdgcn_perm(d2, d3, hi ? X : 0x03020100u);
      *(u32x4*)(a0 + 16 * lane) = u32x4{o1, o2, o3, o4};
      *(u32x4*)(a1 + 16 * lane) = u32x4{o0, o1, o2, o3};
    }
    const u32x4 z4 = {0, 0, 0, 0};
    *(u32x4*)(ch + (lane < 32 ? C_KN0 : C_KN1 - 512) + lane * 16) = z4;
    if (lane < 8) *(u32x4*)(ch + ((lane & 4) ? C_UR1 : C_UR0) + 4096 + (lane & 3) * 16) = z4;
  }
  STAMP(P.ws, blockIdx.x, 2, 4);
  if (!stager) BAR();
  __builtin_amdgcn_s_setprio(0);
  STAMP(P.ws, blockIdx.x, 2, 6); STAMPT(P.ws, 448, 13);

  f32x4 acc[8];
#pragma unroll
  for (int j = 0; j < 8; ++j) acc[j] = f32x4{0.f, 0.f, 0.f, 0.f};
  const char* pa = ch + ((li & 1) ? C_UR1 : C_UR0) + 4088 - 3584 - 8 * (li >> 1) + 16 * g;
  const LDSP char* pa2 = (const LDSP char*)pa + 8;
  asm volatile("" : "+v"(pa2));
  const char* pb = ch + ((li & 1) ? C_KN1 : C_KN0) + 512 + 64 * (li >> 1) + 16 * g;
  u32x4 A[8], Bf[8];
  auto ldA0 = [&](int m, int o) { u32x2 v = *(const u32x2*)(pa + 512 * (7 - m) + o); A[m][0] = v[0]; A[m][1] = v[1]; };
  auto ldA1 = [&](int m, int o) { u32x2 v = *(const LDSP u32x2*)(pa2 + 512 * (7 - m) + o); A[m][2] = v[0]; A[m][3] = v[1]; };
#pragma unroll
  for (int m = 0; m < 8; ++m) {
    if (m < 5) { ldA0(m, 0); ldA1(m, 0); }
    Bf[7 - m] = *(const u32x4*)(pb + 512 * (7 - m));
    SCHED();
  }
#pragma unroll 1
  for (int dl = 0; dl < 8; ++dl) {
    pa -= 64; pa2 -= 64; pb -= 64;
#pragma unroll
    for (int m = 0; m < 8; ++m) {
#pragma unroll
      for (int J = 7; J >= m; --J) {
        acc[J] = mfma_bf(A[m], Bf[J - m], acc[J]);
        const int k = 7 - J;
        if (k == 0) Bf[7 - m] = *(const u32x4*)(pb + 512 * (7 - m));
        else if (m >= 1 && m <= 5 && k == 1) ldA0(m - 1, 0);
        else if (m >= 1 && m <= 5 && k == 2) ldA1(m - 1, 0);
        else if (m == 0 && k <= 6) { if (k & 1) ldA0(5 + (k - 1) / 2, 64); else ldA1(5 + (k - 1) / 2, 64); }
        SCHED();
      }
    }
  }
  STAMP(P.ws, blockIdx.x, 2, 7); STAMPT(P.ws, 448, 14);
  {
    const bool wd = li & 1;
    const u32 yo = WS_YT + (d * SEQ + 32 * (li >> 1) + 8 * g + (wd ? 0 : 4)) * 2;
#pragma unroll
    for (int J = 0; J < 8; ++J) {
      const u32 p0 = pk_bf(acc[J][0], acc[J][1]), p1 = pk_bf(acc[J][2], acc[J][3]);
      const u32 rcv = __builtin_amdgcn_mov_dpp(wd ? p1 : p0, 0xB1, 0xf, 0xf, true);
      st8_wt(rs, yo + 512 * J, wd ? u32x2{p0, rcv} : u32x2{rcv, p1});
    }
  }
  STAMP(P.ws, blockIdx.x, 2, 8);
}

constexpr int G_WSLOT = 20480, G_XR = 102400, G_XSLOT = 16384, G_WO = 151552, G_RED = 159744, G_GW = 161792;
constexpr int G_PS = G_XR + G_XSLOT;

DI void role_gemm(const P1& P, int hg, int p, int s0, char* smem, const char* code) {
  const int tid = threadIdx.x, lane = tid & 63;
  const int w = __builtin_amdgcn_readfirstlane(tid >> 6);
  const int li = lane & 15, g = lane >> 4;
  const rsrc_t rs = ws_rsrc(P.ws);
  STAMP(P.ws, blockIdx.x, 1, 1);
  const int mh = p >> 2, mcq = p & 3;
  const f32x4 zero = {0.f, 0.f, 0.f, 0.f};
  const f32x4 eps4 = {1e-6f, 1e-6f, 1e-6f, 1e-6f};
  const u32x2 z2 = {0u, 0u};
  const int sw = (li >> 1) & 7;
  const char* xr = smem + G_XR + (32 * (w & 3) + li) * 128;
  const int xs0 = (g ^ sw) * 16, xs1 = ((4 + g) ^ sw) * 16;
  const int wrow = (8 * g + (li >> 2)) * 320 + (li & 3) * 8;
  const char* wr_e = smem + wrow + (g & 1) * 32;
  const char* wr_o = smem + wrow - (g & 1) * 32;
  auto wfrag = [&](int un, int ks, int t) {
    const char* a = ((t & 1) ? wr_o : wr_e) + (un % 5) * G_WSLOT + ks * 10240 + t * 32;
    return cat(tr_read(a), tr_read(a + 1280));
  };
  u32x2* red = (u32x2*)(smem + G_RED);

  if (w >= 4) {
    const int qw = w - 4;
    const u32 lds0 = (u32)(size_t)(LDSP char*)smem;
    auto stage_of = [&](int j) { int s = s0 + j; return s >= 10 ? s - 10 : s; };
    u32 xo[4], wc[5];
    u32 hmask = 0;
#pragma unroll
    for (int i = 0; i < 4; ++i) {
      int r = 32 * qw + 8 * i + (lane >> 3);
      int c = (lane & 7) ^ ((r >> 1) & 7);
      xo[i] = ((128 * p + r) * DM + 8 * c) * 2;
    }
    auto issue_x = [&](int j) {
      const char* sb = (const char*)P.u + stage_of(j) * 128;
      const u32 db = lds0 + G_XR + (j % 3) * G_XSLOT + qw * 4096;
#pragma unroll
      for (int i = 0; i < 4; ++i) glds1(sb, db + i * 1024, xo[i]);
    };
    issue_x(0);
#pragma unroll
    for (int i = 0; i < 5; ++i) {
      int id = 64 * (5 * qw + i) + lane;
      int r = id / 20, c = (id - 20 * r) ^ (((r >> 3) & 1) << 1);
      int h = c >= 10;
      wc[i] = (r * NW + 80 * hg + 8 * (c - 10 * h)) * 2;
      hmask |= h << i;
    }
    auto issue_w = [&](int un) {
      const u32 db = lds0 + (un % 5) * G_WSLOT + qw * 5120;
      const int sa = stage_of(un < 10 ? un : 2 * (un - 10));
      const int dlt = un < 10 ? 1280 : (stage_of(2 * (un - 10) + 1) - sa) * (64 * NW * 2);
      const int neg = dlt < 0 ? dlt : 0;
      const u32 dlo = (u32)(-neg), dhi = (u32)(dlt - neg);
      const char* sb = (const char*)P.wqkv + (size_t)sa * (64 * NW * 2) + (un < 10 ? 1280 : 0) + neg;
#pragma unroll
      for (int i = 0; i < 5; ++i) glds1(sb, db + i * 1024, wc[i] + (((hmask >> i) & 1) ? dhi : dlo));
    };
    glds1((const char*)P.gw, lds0 + G_GW + (qw & 1) * 1024, min((qw & 1) * 1024 + lane * 16, 1264));
    issue_w(0); issue_x(1); issue_w(1);
    const bool gate = hg == (p & 7);
    f32x4 ga[2] = {zero, zero};
    u32x4 keep[20][2];
    f32x4 aq[2][5];
#pragma unroll
    for (int c = 0; c < 5; ++c) { aq[0][c] = eps4; aq[1][c] = eps4; }
    auto lstep = [&](auto UN) {
      constexpr int un = decltype(UN)::value;
      if (un < 9) wait_vm<9>(); else wait_vm<7>();
      BAR();
#pragma unroll
      for (int r = 0; r < 2; ++r) {
        keep[2 * un][r] = *(const u32x4*)(xr + (un % 3) * G_XSLOT + r * 2048 + xs0);
        keep[2 * un + 1][r] = *(const u32x4*)(xr + (un % 3) * G_XSLOT + r * 2048 + xs1);
      }
      if (un + 2 < 10) issue_x(un + 2);
      issue_w(un + 2);
      if (gate) {
        const char* gp = smem + G_GW + stage_of(un) * 128 + g * 16;
        const u32x4 g0 = *(const u32x4*)gp, g1 = *(const u32x4*)(gp + 64);
#pragma unroll
        for (int r = 0; r < 2; ++r) { ga[r] = mfma_bf(keep[2 * un][r], g0, ga[r]); ga[r] = mfma_bf(keep[2 * un + 1][r], g1, ga[r]); }
      }
      if (un == 8) {
        const char* wob = (const char*)P.wo + (size_t)(80 * hg + 20 * mh) * (DM * 2) + mcq * 320;
#pragma unroll
        for (int i = 0; i < 2; ++i) {
          int id = min(64 * (2 * qw + i) + lane, 399);
          int r = id / 20, c = id - 20 * r;
          glds1(wob, lds0 + G_WO + (2 * qw + i) * 1024, r * (DM * 2) + c * 16);
        }
      }
    };
    lstep(IC<0>{}); lstep(IC<1>{}); lstep(IC<2>{}); lstep(IC<3>{}); lstep(IC<4>{});
    lstep(IC<5>{}); lstep(IC<6>{}); lstep(IC<7>{}); lstep(IC<8>{}); lstep(IC<9>{});
    {
      u32x4 bq[2][5];
      auto qgrp = [&](auto GG) {
        constexpr int G = decltype(GG)::value, N = G + 1;
        constexpr int kx = 4 * (G / 4) + 2 * (G & 1) + ((G / 2) & 1);
        if (G == 0) {
          wait_vm<0>(); BAR();
          issue_w(12); issue_w(13); issue_w(14);
        }
        if (G == NQE) { wait_vm<0>(); STAMPT(P.ws, 256, 10); BAR(); }
        if (G == 0 || G == NQE) {
#pragma unroll
          for (int n = 0; n < 5; ++n) bq[G & 1][n] = wfrag(10 + G / 4, (G / 2) & 1, 5 * (G & 1) + n);
#pragma unroll
          for (int t = 0; t < 5; ++t) asm volatile("" : "+v"(aq[0][t]), "+v"(aq[1][t]));
          asm volatile("s_nop 1" ::: "memory");
        }
        if (G == NQE) { BAR(); __builtin_amdgcn_s_setprio(1); }
        constexpr bool nxt = N < 20 && N != NQE;
#pragma unroll
        for (int t = 0; t < 5; ++t) {
          const int nt = 5 * (N & 1) + t;
          const char* na = ((nt & 1) ? wr_o : wr_e) + ((10 + N / 4) % 5) * G_WSLOT + ((N / 2) & 1) * 10240 + nt * 32;
          mfma_ip(aq[0][t], bq[G & 1][t], keep[kx][0]);
          if (nxt) { const u32x2 v = tr_read(na); bq[N & 1][t][0] = v[0]; bq[N & 1][t][1] = v[1]; }
          SCHED();
          mfma_ip(aq[1][t], bq[G & 1][t], keep[kx][1]);
          if (nxt) { const u32x2 v = tr_read(na + 1280); bq[N & 1][t][2] = v[0]; bq[N & 1][t][3] = v[1]; }
          SCHED();
        }
      };
      qgrp(IC<0>{}); qgrp(IC<1>{}); qgrp(IC<2>{}); qgrp(IC<3>{}); qgrp(IC<4>{}); qgrp(IC<5>{}); qgrp(IC<6>{});
      qgrp(IC<7>{}); qgrp(IC<8>{}); qgrp(IC<9>{}); qgrp(IC<10>{}); qgrp(IC<11>{}); qgrp(IC<12>{}); qgrp(IC<13>{});
      qgrp(IC<14>{}); qgrp(IC<15>{}); qgrp(IC<16>{}); qgrp(IC<17>{}); qgrp(IC<18>{}); qgrp(IC<19>{});
      asm volatile("s_nop 7\n\ts_nop 7" ::: "memory");
#pragma unroll
      for (int t = 0; t < 5; ++t) asm volatile("" : "+v"(aq[0][t]), "+v"(aq[1][t]));
    }
    u32x2 pq[2][5];
#pragma unroll
    for (int r = 0; r < 2; ++r)
#pragma unroll
      for (int c = 0; c < 5; ++c) pq[r][c] = pk4_bf(phi4(aq[r][c]));
#pragma unroll
    for (int r = 0; r < 2; ++r)
      asm volatile("" : "+v"(pq[r][0]), "+v"(pq[r][1]), "+v"(pq[r][2]), "+v"(pq[r][3]), "+v"(pq[r][4]));
    if (gate) {
      const f32x4 gs = (li & 1) ? ga[1] : ga[0];
      f32x4 go;
#pragma unroll
      for (int r = 0; r < 4; ++r) go[r] = __builtin_amdgcn_rcpf(1.f + __builtin_amdgcn_exp2f(-1.44269504089f * gs[r]));
      if (li < 2) st16_wt(rs, WS_GT + (128 * p + 32 * qw + 16 * li + 4 * g) * 4, __builtin_bit_cast(u32x4, go));
    }
    STAMPT(P.ws, 256, 12); BAR();
    const int hs = li & 3;
    u32x2 zf[5];
#pragma unroll
    for (int c = 0; c < 5; ++c) {
      const int hc = g < c ? c - 1 : c;
      zf[c] = hs == hc ? red[128 + c * 4 + g] : z2;
    }
#pragma unroll
    for (int r = 0; r < 2; ++r) {
      f32x4 den = mfma_bf(cat(zf[0], zf[1]), cat(pq[r][0], pq[r][1]), zero);
      den = mfma_bf(cat(zf[2], zf[3]), cat(pq[r][2], pq[r][3]), den);
      den = mfma_bf(cat(zf[4], z2), cat(pq[r][4], z2), den);
      float rc[4];
#pragma unroll
      for (int j = 0; j < 4; ++j) rc[j] = __builtin_amdgcn_rcpf(den[j] + 1e-6f);
      u32x2 o[5];
#pragma unroll
      for (int c = 0; c < 5; ++c) {
        const float s = c == 0 ? rc[0] : (c == 4 ? rc[3] : (g < c ? rc[c - 1] : rc[c]));
        o[c] = u32x2{pk_bf(bf_lo(pq[r][c][0]) * s, bf_hi(pq[r][c][0]) * s), pk_bf(bf_lo(pq[r][c][1]) * s, bf_hi(pq[r][c][1]) * s)};
      }
      const u32 aqp = WS_AQ + ((8 * p + 2 * qw + r) * 20) * 1024 + lane * 16;
      st16_wt(rs, aqp + (2 * hg) * 1024, cat(o[0], o[1]));
      st16_wt(rs, aqp + (2 * hg + 1) * 1024, cat(o[2], o[3]));
      st8_wt(rs, aqp + (16 + (hg >> 1)) * 1024 + (hg & 1) * 8, o[4]);
    }
    return;
  }

  __builtin_amdgcn_s_setprio(2);
  u32 pf0, pf1;
  asm volatile("global_load_dword %0, %2, %3\n\tglobal_load_dword %1, %4, %3"
               : "=&v"(pf0), "=&v"(pf1) : "v"(tid * 128), "s"(code), "v"(min(32768 + tid * 128, CODE_BYTES - 4)) : "memory");
  const u64 tag = next_tag(P.ws + WS_EP + hg * 64);
  const Tag tg = mk_tag(tag);
  f32x4 acc[2][10];
#pragma unroll
  for (int c = 0; c < 10; ++c) { acc[0][c] = c < 5 ? eps4 : zero; acc[1][c] = acc[0][c]; }
  u32x4 b0[10], b1[10], xa[2], xb[2];
  u32 pso[11];
#pragma unroll
  for (int c = 0; c < 5; ++c) {
    const bool um = li < 4 * c;
    const int jd = g - c + (um ? 5 : 0), jo = jd + (um ? -4 : 4);
    const int eb = (16 * c + li) * 5;
    pso[2 * c] = G_PS + (w * 448 + ((jd >= 0 && jd < 5) ? eb + jd : 420 + li)) * 16;
    pso[2 * c + 1] = G_PS + (w * 448 + ((jo >= 0 && jo < 5) ? eb + jo : 420 + li)) * 16;
  }
  pso[10] = G_PS + (w * 448 + (li < 5 ? 400 + 4 * li + g : 420 + li)) * 16;
#pragma unroll
  for (int c = 0; c < 11; ++c) asm volatile("" : "+v"(pso[c]));
  auto kstep = [&](auto UN) {
    constexpr int un = decltype(UN)::value;
    BAR();
    xa[0] = *(const u32x4*)(xr + (un % 3) * G_XSLOT + xs0);
    xa[1] = *(const u32x4*)(xr + (un % 3) * G_XSLOT + 2048 + xs0);
#pragma unroll
    for (int t = 0; t < 10; ++t) {
      b0[t] = wfrag(un, 0, t);
      if (un > 0) { acc[0][t] = mfma_bf(xb[0], b1[t], acc[0][t]); acc[1][t] = mfma_bf(xb[1], b1[t], acc[1][t]); }
      SCHED();
    }
    xb[0] = *(const u32x4*)(xr + (un % 3) * G_XSLOT + xs1);
    xb[1] = *(const u32x4*)(xr + (un % 3) * G_XSLOT + 2048 + xs1);
#pragma unroll
    for (int t = 0; t < 10; ++t) {
      b1[t] = wfrag(un, 1, t);
      acc[0][t] = mfma_bf(xa[0], b0[t], acc[0][t]); acc[1][t] = mfma_bf(xa[1], b0[t], acc[1][t]);
      SCHED();
    }
  };
  kstep(IC<0>{}); kstep(IC<1>{}); kstep(IC<2>{}); kstep(IC<3>{}); kstep(IC<4>{});
  asm volatile("s_waitcnt vmcnt(0)" : "+v"(pf0), "+v"(pf1) :: "memory");
  kstep(IC<5>{}); kstep(IC<6>{}); kstep(IC<7>{}); kstep(IC<8>{}); kstep(IC<9>{});
  BAR();
  STAMP(P.ws, blockIdx.x, 1, 2);
#pragma unroll
  for (int t = 0; t < 10; ++t) { acc[0][t] = mfma_bf(xb[0], b1[t], acc[0][t]); acc[1][t] = mfma_bf(xb[1], b1[t], acc[1][t]); }
  {
    u32x4 fr[10];
#pragma unroll
    for (int t = 0; t < 10; ++t)
      fr[t] = t < 5 ? cat(pk4_bf(phi4(acc[0][t])), pk4_bf(phi4(acc[1][t]))) : cat(pk4_bf(acc[0][t]), pk4_bf(acc[1][t]));
    const u32x4 ones = {0x3f803f80u, 0x3f803f80u, 0x3f803f80u, 0x3f803f80u};
    f32x4 zsel = zero;
#pragma unroll
    for (int c = 0; c < 5; ++c) {
      const f32x4 dd = mfma_bf(fr[5 + c], fr[c], zero);
      f32x4 dm = zero, dp = zero;
      if (c > 0) dm = mfma_bf(fr[4 + c], fr[c], zero);
      if (c < 4) dp = mfma_bf(fr[6 + c], fr[c], zero);
      const f32x4 dz = mfma_bf(fr[c], ones, zero);
      *(f32x4*)(smem + pso[2 * c]) = dd;
      *(f32x4*)(smem + pso[2 * c + 1]) = c == 0 ? dp : (c == 4 ? dm : (li < 4 * c ? dm : dp));
      if (c == 0 || li == c) zsel = dz;
    }
    *(f32x4*)(smem + pso[10]) = zsel;
  }
  STAMPT(P.ws, 0, 13); BAR();
  {
    const f32x4* ps = (const f32x4*)(smem + G_PS);
    const int e1 = min(tid + 256, 423);
    const f32x4 v0 = (ps[tid] + ps[448 + tid]) + (ps[896 + tid] + ps[1344 + tid]);
    const f32x4 v1 = (ps[e1] + ps[448 + e1]) + (ps[896 + e1] + ps[1344 + e1]);
    const u32 o = WS_KV + (hg * 16 + p) * KV_STR;
    st16_dev(rs, o + tid * 16, tag4(v0, tg));
    if (w < 3) st16_dev(rs, o + e1 * 16, tag4(v1, tg));
  }
  STAMP(P.ws, blockIdx.x, 1, 3);
  BAR();
  u32x4 wf[3];
  {
    const char* lo = smem + G_WO + (4 * g + (li >> 2)) * 320 + (li & 3) * 8;
    const char* hi = smem + G_WO + (16 + (li >> 2)) * 320 + (li & 3) * 8;
#pragma unroll
    for (int k = 0; k < 3; ++k) {
      const int t = min(w + 4 * k, 9);
      wf[k] = cat(tr_read(lo + t * 32), tr_read(hi + t * 32));
    }
  }
  __builtin_amdgcn_s_setprio(0);
  {
    const int he = min(32 * w + (lane & 31), 99), ze = min(tid >> 3, 19);
    const u32 h1o = WS_KV + (hg * 16 + 8 * (lane >> 5)) * KV_STR + (mh * 100 + he) * 16;
    const u32 zo = WS_KV + (hg * 16 + 2 * (tid & 7)) * KV_STR + (400 + ze) * 16;
    u32x4 h1[8], zv[2];
    for (u32 spin = 0; spin < SPIN_MAX; ++spin) {
#pragma unroll
      for (int i = 0; i < 8; ++i) h1[i] = ld16_dev(rs, h1o + KV_STR * i);
      zv[0] = ld16_dev(rs, zo); zv[1] = ld16_dev(rs, zo + KV_STR);
      u32 bad = tag_bad(zv[0], tg) | tag_bad(zv[1], tg);
#pragma unroll
      for (int i = 0; i < 8; ++i) bad |= tag_bad(h1[i], tg);
      if (__all((bad & 0xfff) == 0)) break;
      asm volatile("" ::: "memory");
    }
    __builtin_amdgcn_s_setprio(2);
    STAMPT(P.ws, 0, 14);
    f32x4 ks = ((untag(h1[0]) + untag(h1[1])) + (untag(h1[2]) + untag(h1[3]))) +
               ((untag(h1[4]) + untag(h1[5])) + (untag(h1[6]) + untag(h1[7])));
    f32x4 zs = untag(zv[0]) + untag(zv[1]);
#pragma unroll
    for (int r = 0; r < 4; ++r) {
      const float kr = ks[r];
      const auto sw2 = __builtin_amdgcn_permlane32_swap(__float_as_uint(kr), __float_as_uint(kr), false, false);
      ks[r] = __uint_as_float(sw2[0]) + __uint_as_float(sw2[1]);
      zs[r] = dpp_add<0x141>(dpp_add<0x4E>(dpp_add<0xB1>(zs[r])));
    }
    if (lane < 32) red[32 * w + lane] = pk4_bf(ks);
    if ((tid & 7) == 0 && tid < 160) red[128 + ze] = pk4_bf(zs);
  }
  BAR();
  STAMP(P.ws, blockIdx.x, 1, 5);
  if (p == 15 && tid == 0) st8_dev(P.ws + WS_EP + hg * 64, tag);
  {
    const int ib = 16 + (li & 3);
    const u32x4 a0 = cat(red[li * 5 + g], g == 0 ? red[li * 5 + 4] : z2);
    const u32x4 a1 = cat(red[ib * 5 + g], g == 0 ? red[ib * 5 + 4] : z2);
    auto dst = [&](int qi) {
      const int mc = qi >> 2;
      const int ksl = mc < 4 ? 2 * hg + (mc >> 1) : 16 + (hg >> 1), hf = mc < 4 ? (mc & 1) : (hg & 1);
      return (u32)(ksl * 1024 + (li + 16 * (qi & 3)) * 16 + hf * 8);
    };
    const u32 cb = WS_MW + (10 * mcq + w) * 20480;
    const u32 d0 = dst(5 * mh + g), d1 = dst(5 * mh + 4);
    u32x2 mb = z2;
#pragma unroll
    for (int k = 0; k < 3; ++k) {
      const u32x2 m0 = pk4_bf(mfma_bf(a0, wf[k], zero)), m1 = pk4_bf(mfma_bf(a1, wf[k], zero));
      if (k < 2 || w < 2) st8_wt(rs, cb + k * (4 * 20480) + d0, m0);
      if (g == k) mb = m1;
    }
    if (g < 2 || (g == 2 && w < 2)) st8_wt(rs, cb + g * (4 * 20480) + d1, mb);
  }
  STAMP(P.ws, blockIdx.x, 1, 6);
}

__global__ __launch_bounds__(512, 2) void k1(P1 P) {
  extern __shared__ __attribute__((aligned(16))) char smem[];
  const char* code;
  asm volatile("s_getpc_b64 %0" : "=s"(code));
  const int b = blockIdx.x, x = b & 7, r = b >> 3;
  if (r < 16) {
    const int hl = r & 3, pl = r >> 2;
    role_gemm(P, (x & 1) * 4 + hl, (x >> 1) * 4 + pl, ((5 * (pl + hl)) / 2 + (hl & 1)) % 10, smem, code);
  } else {
    role_conv(P, x * 10 + (r - 16), smem);
  }
}

__global__ __launch_bounds__(256, 2) void k2(const char* ws, u16* out) {
  extern __shared__ __attribute__((aligned(16))) char smem[];
  const int tid = threadIdx.x, lane = tid & 63, li = lane & 15, g = lane >> 4;
  const int w = __builtin_amdgcn_readfirstlane(tid >> 6);
  const int b = blockIdx.x, x = b & 7, r_ = b >> 3;
  const int rl = r_ >> 2, cl = r_ & 3;
  const int rg = (x >> 1) * 8 + rl, cg = (x & 1) * 4 + cl;
  STAMP(ws, 1024 + blockIdx.x, 5, 1);
  const int s0 = (rl * 5 / 2 + cl * 5) % 20;
  const int rt = rg * 4 + w;
  const int t0 = rt * 16 + 4 * g;
  u32x2 ya[5];
#pragma unroll
  for (int c = 0; c < 5; ++c) ya[c] = *(const u32x2*)((const u16*)(ws + WS_YT) + (size_t)(cg * 80 + c * 16 + li) * SEQ + t0);
  const f32x4 gt = *(const f32x4*)((const float*)(ws + WS_GT) + t0);
  const char* ab = ws + WS_AQ + (size_t)rt * 20480;
  const u32 lds0 = (u32)(size_t)(LDSP char*)smem;
  const char* mb = ws + WS_MW + (size_t)cg * 5 * 20480;
  const u32 lo16 = lane * 16;
  u32x4 a[20];
  auto issue = [&](int q, int i) {
    if (i < 5) {
      int ks = 4 * q + w + s0; ks = ks >= 20 ? ks - 20 : ks;
      glds1(mb + ks * 1024 + i * 20480, lds0 + (4 * q + w) * 5120 + i * 1024, lo16);
    } else {
      int ks = 4 * q + (i - 5) + s0; ks = ks >= 20 ? ks - 20 : ks;
      asm volatile("global_load_dwordx4 %0, %1, %2" : "=v"(a[4 * q + i - 5]) : "v"(lo16), "s"(ab + ks * 1024) : "memory");
    }
  };
#pragma unroll
  for (int q = 0; q < 3; ++q)
#pragma unroll
    for (int i = 0; i < 9; ++i) issue(q, i);
  f32x4 acc[5];
#pragma unroll
  for (int c = 0; c < 5; ++c) acc[c] = f32x4{0.f, 0.f, 0.f, 0.f};
  const char* mrd = smem + lane * 16;
#pragma unroll
  for (int q = 0; q < 5; ++q) {
    if (q < 3) wait_vm_bar<18>(); else if (q == 3) wait_vm_bar<9>(); else wait_vm_bar<0>();
    asm volatile("" : "+v"(a[4 * q]), "+v"(a[4 * q + 1]), "+v"(a[4 * q + 2]), "+v"(a[4 * q + 3]));
    u32x4 bf[20];
#pragma unroll
    for (int i = 0; i < 20; ++i) bf[i] = *(const u32x4*)(mrd + (q * 20 + i) * 1024);
    SCHED();
#pragma unroll
    for (int i = 0; i < 20; ++i) {
      acc[i % 5] = mfma_bf(a[4 * q + i / 5], bf[i], acc[i % 5]);
      if (q + 3 < 5 && (i & 1) && i < 18) { issue(q + 3, i >> 1); SCHED(); }
    }
  }
  STAMP(ws, 1024 + blockIdx.x, 5, 3);
  const bool odd = li & 1;
  u16* op = out + (size_t)(t0 + (odd ? 2 : 0)) * DM + cg * 80 + (li & ~1);
#pragma unroll
  for (int c = 0; c < 5; ++c) {
    float v0 = gt[0] * bf_lo(ya[c][0]) + (1.f - gt[0]) * acc[c][0];
    float v1 = gt[1] * bf_hi(ya[c][0]) + (1.f - gt[1]) * acc[c][1];
    float v2 = gt[2] * bf_lo(ya[c][1]) + (1.f - gt[2]) * acc[c][2];
    float v3 = gt[3] * bf_hi(ya[c][1]) + (1.f - gt[3]) * acc[c][3];
    u32 p01 = pk_bf(v0, v1), p23 = pk_bf(v2, v3);
    u32 send = odd ? p01 : p23, keep = odd ? p23 : p01;
    u32 recv = __builtin_amdgcn_mov_dpp(send, 0xB1, 0xf, 0xf, true);
    u32 lo = odd ? recv : keep, hi = odd ? keep : recv;
    *(u32*)(op + c * 16) = __builtin_amdgcn_perm(hi, lo, 0x05040100u);
    *(u32*)(op + c * 16 + DM) = __builtin_amdgcn_perm(hi, lo, 0x07060302u);
  }
  STAMP(ws, 1024 + blockIdx.x, 5, 4);
}

extern "C" void kernel_launch(void* const* d_in, const int* in_sizes, int n_in, void* d_out, int out_size,
                              void* d_ws, size_t ws_size, hipStream_t stream) {
  P1 P;
  P.u = (const u16*)d_in[0]; P.are = (const u16*)d_in[1]; P.aim = (const u16*)d_in[2];
  P.bre = (const u16*)d_in[3]; P.bim = (const u16*)d_in[4]; P.cre = (const u16*)d_in[5]; P.cim = (const u16*)d_in[6];
  P.logdt = (const u16*)d_in[7]; P.dskip = (const u16*)d_in[8]; P.wqkv = (const u16*)d_in[10];
  P.wo = (const u16*)d_in[11]; P.gw = (const u16*)d_in[12]; P.ws = (char*)d_ws;
  (void)hipFuncSetAttribute((const void*)k1, hipFuncAttributeMaxDynamicSharedMemorySize, 163840);
  (void)hipFuncSetAttribute((const void*)k2, hipFuncAttributeMaxDynamicSharedMemorySize, 102400);
  k1<<<208, 512, 163840, stream>>>(P);
  k2<<<256, 256, 102400, stream>>>((const char*)d_ws, (u16*)d_out);
}
```

```cpp
#include <hip/hip_runtime.h>
#include <stdint.h>

typedef unsigned short u16;
typedef unsigned int u32;
typedef unsigned long long u64;
typedef __attribute__((ext_vector_type(8))) __bf16 bf16x8;
typedef __attribute__((ext_vector_type(2))) __bf16 bf16x2;
typedef __attribute__((ext_vector_type(8))) _Float16 f16x8;
typedef __attribute__((ext_vector_type(2))) _Float16 f16x2;
typedef __attribute__((ext_vector_type(4))) short s16x4;
typedef __attribute__((ext_vector_type(4))) float f32x4;
typedef __attribute__((ext_vector_type(2))) float f32x2;
typedef __attribute__((ext_vector_type(4))) u32 u32x4;
typedef __attribute__((ext_vector_type(2))) u32 u32x2;
#define LDSP __attribute__((address_space(3)))
#define GLBP __attribute__((address_space(1)))
#define DI __device__ __forceinline__

constexpr int SEQ = 2048, DM = 640, NW = 1920;

constexpr u32 WS_AQ = 0;
constexpr u32 WS_MW = WS_AQ + 128u * 20 * 1024;
constexpr u32 WS_YT = WS_MW + 40u * 20 * 1024;
constexpr u32 WS_GT = WS_YT + 640u * 2048 * 2;
constexpr u32 WS_KV = WS_GT + 2048u * 4;
constexpr u32 KV_STR = 424 * 16;
constexpr u32 WS_EP = WS_KV + 128u * KV_STR;
constexpr u32 WS_END = WS_EP + 8u * 64;
constexpr u64 EPOCH_STEP = 0x9E37001001001001ull;
constexpr u32 SPIN_MAX = 200000;
constexpr int NQE = 4;
constexpr int CODE_BYTES = 38000;

DI float bf_lo(u32 v) { return __uint_as_float(v << 16); }
DI float bf_hi(u32 v) { return __uint_as_float(v & 0xffff0000u); }
DI u32 pk_bf(float a, float b) { f32x2 v = {a, b}; return __builtin_bit_cast(u32, __builtin_convertvector(v, bf16x2)); }
DI u32 pk_h(float a, float b) { f32x2 v = {a, b}; return __builtin_bit_cast(u32, __builtin_convertvector(v, f16x2)); }
DI u32x2 pk4_bf(f32x4 v) { return u32x2{pk_bf(v[0], v[1]), pk_bf(v[2], v[3])}; }
DI bf16x8 as_bf8(u32x4 v) { return __builtin_bit_cast(bf16x8, v); }
DI f16x8 as_h8(u32x4 v) { return __builtin_bit_cast(f16x8, v); }
DI u32x4 cat(u32x2 a, u32x2 b) { return u32x4{a[0], a[1], b[0], b[1]}; }
DI f32x4 mfma_bf(u32x4 a, u32x4 b, f32x4 c) { return __builtin_amdgcn_mfma_f32_16x16x32_bf16(as_bf8(a), as_bf8(b), c, 0, 0, 0); }
DI void mfma_ip(f32x4& c, u32x4 a, u32x4 b) { asm volatile("v_mfma_f32_16x16x32_bf16 %0, %1, %2, %0" : "+v"(c) : "v"(a), "v"(b)); }
DI f32x4 mfma_h(u32x4 a, u32x4 b, f32x4 c) { return __builtin_amdgcn_mfma_f32_16x16x32_f16(as_h8(a), as_h8(b), c, 0, 0, 0); }
DI float exp2_clamp(float x) { float d; asm("v_exp_f32 %0, %1 clamp" : "=v"(d) : "v"(x)); return d; }
DI float max_raw(float a, float b) { float d; asm("v_max_f32 %0, %1, %2" : "=v"(d) : "v"(a), "v"(b)); return d; }
DI f32x4 phi4(f32x4 v) {
  const f32x4 s = v * 1.44269504089f;
  f32x4 o;
#pragma unroll
  for (int i = 0; i < 4; ++i) {
    const float si = s[i];
    o[i] = __builtin_fmaf(max_raw(si, 1.44269504e-6f), 0.69314718056f, exp2_clamp(si));
  }
  return o;
}
DI u32x2 tr_read(const char* l) {
  s16x4 t = __builtin_amdgcn_ds_read_tr16_b64_v4i16((LDSP s16x4*)l);
  return __builtin_bit_cast(u32x2, t);
}
template <int CTRL> DI float dpp_add(float v) {
  return v + __int_as_float(__builtin_amdgcn_mov_dpp(__float_as_int(v), CTRL, 0xf, 0xf, true));
}
template <int N> struct IC { static constexpr int value = N; };
#define BAR() asm volatile("s_waitcnt lgkmcnt(0)\n\ts_barrier" ::: "memory")
#define MEMB() asm volatile("" ::: "memory")
#define SCHED() __builtin_amdgcn_sched_barrier(0)
template <int N> DI void wait_vm() { asm volatile("s_waitcnt vmcnt(%0)" ::"n"(N) : "memory"); }
template <int N> DI void wait_vm_bar() { asm volatile("s_waitcnt vmcnt(%0) lgkmcnt(0)\n\ts_barrier" ::"n"(N) : "memory"); }

DI void glds1(const char* sbase, u32 dst, u32 off) {
  asm volatile("s_mov_b32 m0, %1\n\ts_nop 0\n\tglobal_load_lds_dwordx4 %2, %0" :: "s"(sbase), "s"(dst), "v"(off) : "memory");
}
typedef __amdgpu_buffer_rsrc_t rsrc_t;
DI rsrc_t ws_rsrc(char* ws) { return __builtin_amdgcn_make_buffer_rsrc((void*)ws, (short)0, (int)WS_END, 0x00020000); }
DI void st16_dev(rsrc_t r, u32 off, u32x4 v) { __builtin_amdgcn_raw_buffer_store_b128(v, r, off, 0, 16); }
DI void st8_wt(rsrc_t r, u32 off, u32x2 v) { __builtin_amdgcn_raw_buffer_store_b64(v, r, off, 0, 16); }
DI u32x4 ld16_dev(rsrc_t r, u32 off) { return __builtin_amdgcn_raw_buffer_load_b128(r, off, 0, 16); }
DI void st8_dev(char* p, u64 v) { __hip_atomic_store((GLBP u64*)p, v, __ATOMIC_RELAXED, __HIP_MEMORY_SCOPE_AGENT); }
struct Tag { u32 t0, t1, t2, t3; };
DI Tag mk_tag(u64 t) { Tag g; g.t0 = (u32)t & 0xfff; g.t1 = (u32)(t >> 12) & 0xfff; g.t2 = (u32)(t >> 24) & 0xfff; g.t3 = (u32)(t >> 36) & 0xfff; return g; }
DI u32x4 tag4(f32x4 v, Tag t) {
  u32x4 b = __builtin_bit_cast(u32x4, v);
  return u32x4{((b[0] + 0x800u) & 0xfffff000u) | t.t0, ((b[1] + 0x800u) & 0xfffff000u) | t.t1,
               ((b[2] + 0x800u) & 0xfffff000u) | t.t2, ((b[3] + 0x800u) & 0xfffff000u) | t.t3};
}

DI u64 next_tag(const char* p) {
  u64 e;
  asm volatile("s_load_dwordx2 %0, %1, 0x0\n\ts_waitcnt lgkmcnt(0)" : "=s"(e) : "s"(p) : "memory");
  return e + EPOCH_STEP;
}

struct P1 {
  const u16 *u, *are, *aim, *bre, *bim, *cre, *cim, *logdt, *dskip, *wqkv, *wo, *gw;
  char* ws;
};

DI const char* pf_addr(const P1& P, int x, int l) {
  const int st = l / 1152, o = l - st * 1152;
  if (st < 10) {
    if (o < 512) return (const char*)P.u + (size_t)((x >> 1) * 512 + o) * 1280 + st * 128;
    const int k = (o - 512) / 10, c = (o - 512) - 10 * k;
    return (const char*)P.wqkv + (st * 64 + k) * 3840 + (1 + c / 5) * 1280 + (x & 1) * 640 + (c % 5) * 128;
  }
  const int m = min(l - 11520, 3199), k = m / 5;
  return (const char*)P.wqkv + k * 3840 + (x & 1) * 640 + (m - 5 * k) * 128;
}

constexpr int C_UR0 = 256, C_UR1 = 4480, C_KN0 = 8704, C_KN1 = 13344, C_CH = 18176, C_SSTR = 544;

struct cpx { float r0, r1, i0, i1; };
DI cpx cmul(cpx a, cpx b) {
  cpx o;
  f32x2 ar = {a.r0, a.r1}, ai = {a.i0, a.i1}, br = {b.r0, b.r1}, bi = {b.i0, b.i1};
  f32x2 r = ar * br - ai * bi, i = ar * bi + ai * br;
  o.r0 = r[0]; o.r1 = r[1]; o.i0 = i[0]; o.i1 = i[1];
  return o;
}
DI cpx cexp_l(f32x2 er, f32x2 th, float l) {
  cpx o;
  float m0 = __builtin_amdgcn_exp2f(er[0] * l), m1 = __builtin_amdgcn_exp2f(er[1] * l);
  float p0 = __builtin_amdgcn_fractf(th[0] * l), p1 = __builtin_amdgcn_fractf(th[1] * l);
  o.r0 = m0 * __builtin_amdgcn_cosf(p0); o.r1 = m1 * __builtin_amdgcn_cosf(p1);
  o.i0 = m0 * __builtin_amdgcn_sinf(p0); o.i1 = m1 * __builtin_amdgcn_sinf(p1);
  return o;
}
DI void put_row(char* srow, cpx v) {
  u32x2 w = {pk_h(v.r0, v.i0), pk_h(v.r1, v.i1)};
  *(u32x2*)srow = w;
}
DI f32x4 kgen_tile(const char* sread, const u32x4 (&xf)[8]) {
  f32x4 a0 = {0.f, 0.f, 0.f, 0.f}, a1 = a0;
  MEMB();
#pragma unroll
  for (int s = 0; s < 8; s += 2) {
    a0 = mfma_h(*(const u32x4*)(sread + 64 * s), xf[s], a0);
    a1 = mfma_h(*(const u32x4*)(sread + 64 * s + 64), xf[s + 1], a1);
  }
  MEMB();
  return a0 + a1;
}

DI void role_conv(const P1& P, int cb, char* smem) {
  const int tid = threadIdx.x, lane = tid & 63;
  const int w = __builtin_amdgcn_readfirstlane(tid >> 6);
  const int d0 = cb * 8, d = d0 + w;
  char* ch = smem + w * C_CH;
  const int li = lane & 15, g = lane >> 4;
  const int pi = d * 128 + 2 * lane;
  u32 w_are = *(const u32*)(P.are + pi), w_aim = *(const u32*)(P.aim + pi);
  u32 w_bre = *(const u32*)(P.bre + pi), w_bim = *(const u32*)(P.bim + pi);
  u32 w_cre = *(const u32*)(P.cre + pi), w_cim = *(const u32*)(P.cim + pi);
  const u16 w_ldt = P.logdt[d], w_dsk = P.dskip[d];
  SCHED();
  const bool early = w >= 4;
  u32x4 ur[5];
  const u16* up = P.u + (size_t)(tid * 4) * DM + d0;
  auto gather = [&](int lo, int hi, bool on) {
    if (on) {
#pragma unroll
      for (int r = lo; r < hi; ++r) ur[r] = *(const u32x4*)((r == 0 && tid == 0) ? up : up + (r - 1) * DM);
    }
    SCHED();
  };
  ur[0] = *(const u32x4*)(early ? (const char*)(up - DM) : pf_addr(P, cb / 10, 3072 + (cb % 10) * 256 + tid));
  SCHED();
  u32 w_ld = w_ldt, w_dk = w_dsk;
  asm volatile("" : "+v"(w_are), "+v"(w_aim), "+v"(w_bre), "+v"(w_bim), "+v"(w_cre), "+v"(w_cim), "+v"(w_ld), "+v"(w_dk));
  gather(1, 2, early);
  float dt = expf(bf_lo(w_ld));
  const float dsk = bf_lo(w_dk);
  dt = bf_lo(pk_bf(dt, dt));
  f32x2 er = {bf_lo(w_are), bf_hi(w_are)}, th = {bf_lo(w_aim), bf_hi(w_aim)};
  er = (er * dt) * 1.44269504089f;
  th = (th * dt) * 0.15915494309f;
  float mu = 0.f;
  if (__any(er[0] > 0.f || er[1] > 0.f)) {
    mu = fmaxf(fmaxf(er[0], er[1]), 0.f);
#pragma unroll
    for (int o = 32; o; o >>= 1) mu = fmaxf(mu, __shfl_xor(mu, o));
    er -= mu;
  }
  cpx B, C;
  B.r0 = bf_lo(w_bre) * dt; B.r1 = bf_hi(w_bre) * dt; B.i0 = bf_lo(w_bim) * dt; B.i1 = bf_hi(w_bim) * dt;
  C.r0 = bf_lo(w_cre); C.r1 = bf_hi(w_cre); C.i0 = bf_lo(w_cim); C.i1 = bf_hi(w_cim);
  cpx c = cmul(C, B);
  float mx = fmaxf(fmaxf(fabsf(c.r0), fabsf(c.r1)), fmaxf(fabsf(c.i0), fabsf(c.i1)));
  u32 me = __float_as_uint(mx) & 0x7f800000u;
  me = max(me, (u32)__builtin_amdgcn_mov_dpp((int)me, 0xB1, 0xf, 0xf, true));
  me = max(me, (u32)__builtin_amdgcn_mov_dpp((int)me, 0x4E, 0xf, 0xf, true));
  me = max(me, (u32)__builtin_amdgcn_mov_dpp((int)me, 0x141, 0xf, 0xf, true));
  me = max(me, (u32)__builtin_amdgcn_mov_dpp((int)me, 0x140, 0xf, 0xf, true));
  const u32 mall = max(max((u32)__builtin_amdgcn_readlane((int)me, 0), (u32)__builtin_amdgcn_readlane((int)me, 16)),
                       max((u32)__builtin_amdgcn_readlane((int)me, 32), (u32)__builtin_amdgcn_readlane((int)me, 48)));
  const u32 mexp = min(max(mall, 0x0c000000u), 0x7c000000u);
  {
    const float inv = __uint_as_float(0x82000000u - mexp);
    c.r0 *= inv; c.r1 *= inv; c.i0 *= inv; c.i1 *= inv;
  }
  const float kscale = __uint_as_float(mexp - 0x07000000u);

  const cpx E1 = cexp_l(er, th, 1.f), E16 = cexp_l(er, th, 16.f), E256 = cexp_l(er, th, 256.f);
  char* sc = ch + C_KN0;
  char* swr = sc + lane * 8;
  const char* srd = sc + li * C_SSTR + g * 16;
  gather(2, 4, early);
  u32x4 xf[8];
  {
    cpx v; v.r0 = 256.f; v.r1 = 256.f; v.i0 = 0.f; v.i1 = 0.f;
    cpx e = E16; e.i0 = -e.i0; e.i1 = -e.i1;
#pragma unroll 4
    for (int o = 0; o < 16; ++o) { put_row(swr + o * C_SSTR, v); v = cmul(v, e); }
    MEMB();
#pragma unroll
    for (int s = 0; s < 8; ++s) xf[s] = *(const u32x4*)(srd + 64 * s);
    MEMB();
  }
  gather(4, 5, early);
  f32x4 k0, k1, k2;
  {
    cpx v = c;
#pragma unroll 4
    for (int o = 0; o < 16; ++o) { put_row(swr + o * C_SSTR, v); v = cmul(v, E1); }
    k0 = kgen_tile(srd, xf);
  }
  const cpx E2 = cmul(E1, E1), E4 = cmul(E2, E2), E8 = cmul(E4, E4);
  cpx G = cmul(c, E256);
  {
    cpx v = G;
#pragma unroll 4
    for (int o = 0; o < 8; ++o) { put_row(swr + o * C_SSTR, v); v = cmul(v, E2); }
#pragma unroll
    for (int m = 0; m < 2; ++m) {
      G = cmul(G, E256); v = G;
#pragma unroll
      for (int o = 0; o < 4; ++o) { put_row(swr + (8 + 4 * m + o) * C_SSTR, v); v = cmul(v, E4); }
    }
    k1 = kgen_tile(srd, xf);
  }
  const rsrc_t rs = ws_rsrc(P.ws);
  {
#pragma unroll
    for (int m = 0; m < 4; ++m) {
      G = cmul(G, E256);
      put_row(swr + (2 * m) * C_SSTR, G);
      put_row(swr + (2 * m + 1) * C_SSTR, cmul(G, E8));
    }
    G = cmul(G, E256);
    put_row(swr + 8 * C_SSTR, G);
    k2 = kgen_tile(srd, xf);
  }
  k0 *= kscale; k1 *= kscale; k2 *= kscale;
  if (__builtin_amdgcn_readfirstlane(__float_as_uint(mu)) != 0) {
#pragma unroll
    for (int r = 0; r < 4; ++r) {
      const int l0 = 16 * li + 4 * g + r;
      const int l1 = 16 * li + (g < 2 ? 256 + 8 * g + 2 * r : 256 * g + 4 * r);
      const int l2 = 16 * li + 1024 + 512 * g + 256 * (r >> 1) + 8 * (r & 1);
      k0[r] *= __builtin_amdgcn_exp2f(mu * (float)l0);
      k1[r] *= __builtin_amdgcn_exp2f(mu * (float)l1);
      k2[r] *= __builtin_amdgcn_exp2f(mu * (float)l2);
    }
  }
  if (lane == 0) k0[0] += dsk;

  gather(0, 5, !early);
  {
    char* kc = ch + C_KN0 + 16;
    *(u32x2*)(kc + 2 * (16 * li + 4 * g)) = pk4_bf(k0);
    *(u32x2*)(kc + 2 * (g < 2 ? 256 + 8 * li + 4 * g : 256 + 64 * g + 4 * li)) = pk4_bf(k1);
    {
      const u32x2 p2 = pk4_bf(k2);
      const int i2 = g < 2 ? 512 + 64 * g + 2 * li : (lane == 32 ? 640 : 704);
      *(u32*)(kc + 2 * i2) = p2[0];
      *(u32*)(kc + 2 * (g < 2 ? i2 + 32 : -2)) = g < 2 ? p2[1] : 0u;
    }
    MEMB();
    const u32 s30 = *(const u32*)(kc + 1024 + 4 * lane), s31 = *(const u32*)(kc + 1028 + 4 * lane);
    const u32 s20 = *(const u32*)(kc + 768 + 4 * lane), s21 = *(const u32*)(kc + 772 + 4 * lane);
    const bool hi = lane >= 32;
    const char* q = kc + (hi ? 256 + 8 * lane : 16 * lane);
    const u32 dm = *(const u32*)(q - 4), d0 = *(const u32*)q, d1 = *(const u32*)(q + 4), d2 = *(const u32*)(q + 8);
    const u32 d3 = *(const u32*)(q + (hi ? 4 : 12));
    const u32 k510 = *(const u16*)(kc + 766);
    MEMB();
    char* a0 = ch + C_KN0 + 512;
    char* a1 = ch + C_KN1 + 512;
    const u32 LL = 0x01000100u, HH = 0x03020302u;
    {
      const u32 a = __builtin_amdgcn_perm(s30, s30, LL), c = __builtin_amdgcn_perm(s30, s30, HH), e = __builtin_amdgcn_perm(s31, s31, LL);
      *(u32x4*)(a0 + 2048 + 32 * lane) = u32x4{a, a, s30, c};
      *(u32x4*)(a0 + 2064 + 32 * lane) = u32x4{c, c, e, e};
      *(u32x4*)(a1 + 2048 + 32 * lane) = u32x4{a, a, a, s30};
      *(u32x4*)(a1 + 2064 + 32 * lane) = u32x4{c, c, c, e};
    }
    {
      const u32 a = __builtin_amdgcn_perm(s20, s20, LL), c = __builtin_amdgcn_perm(s20, s20, HH), e = __builtin_amdgcn_perm(s21, s21, LL);
      *(u32x4*)(a0 + 1024 + 16 * lane) = u32x4{a, s20, c, e};
      *(u32x4*)(a1 + 1024 + 16 * lane) = u32x4{lane == 0 ? ((a & 0xffff0000u) | k510) : a, a, s20, c};
    }
    {
      const u32 X = 0x05040302u;
      const u32 o0 = __builtin_amdgcn_perm(d0, dm, lane > 32 ? X : 0x03020100u);
      const u32 o1 = __builtin_amdgcn_perm(d0, d0, hi ? LL : 0x03020100u);
      const u32 o2 = __builtin_amdgcn_perm(d1, d0, hi ? X : 0x07060504u);
      const u32 o3 = __builtin_amdgcn_perm(d2, d1, hi ? LL : 0x07060504u);
      const u32 o4 = __builtin_amdgcn_perm(d2, d3, hi ? X : 0x03020100u);
      *(u32x4*)(a0 + 16 * lane) = u32x4{o1, o2, o3, o4};
      *(u32x4*)(a1 + 16 * lane) = u32x4{o0, o1, o2, o3};
    }
    const u32x4 z4 = {0, 0, 0, 0};
    *(u32x4*)(ch + (lane < 32 ? C_KN0 : C_KN1 - 512) + lane * 16) = z4;
    if (lane < 8) *(u32x4*)(ch + ((lane & 4) ? C_UR1 : C_UR0) + 4096 + (lane & 3) * 16) = z4;
  }
  {
    if (tid == 0) ur[0] = u32x4{0, 0, 0, 0};
    char* ub = smem + 4088 - 8 * tid;
#pragma unroll
    for (int cc = 0; cc < 8; ++cc) {
      const u32 sel = (cc & 1) ? 0x07060302u : 0x05040100u;
      const int k = cc >> 1;
      *(u32x2*)(ub + cc * C_CH + C_UR1) = u32x2{__builtin_amdgcn_perm(ur[3][k], ur[4][k], sel), __builtin_amdgcn_perm(ur[1][k], ur[2][k], sel)};
      *(u32x2*)(ub + cc * C_CH + C_UR0) = u32x2{__builtin_amdgcn_perm(ur[2][k], ur[3][k], sel), __builtin_amdgcn_perm(ur[0][k], ur[1][k], sel)};
    }
  }
  BAR();

  f32x4 acc[8];
#pragma unroll
  for (int j = 0; j < 8; ++j) acc[j] = f32x4{0.f, 0.f, 0.f, 0.f};
  const char* pa = ch + ((li & 1) ? C_UR1 : C_UR0) + 4088 - 3584 - 8 * (li >> 1) + 16 * g;
  const LDSP char* pa2 = (const LDSP char*)pa + 8;
  asm volatile("" : "+v"(pa2));
  const char* pb = ch + ((li & 1) ? C_KN1 : C_KN0) + 512 + 64 * (li >> 1) + 16 * g;
  u32x4 A[8], Bf[8];
  auto ldA0 = [&](int m, int o) { u32x2 v = *(const u32x2*)(pa + 512 * (7 - m) + o); A[m][0] = v[0]; A[m][1] = v[1]; };
  auto ldA1 = [&](int m, int o) { u32x2 v = *(const LDSP u32x2*)(pa2 + 512 * (7 - m) + o); A[m][2] = v[0]; A[m][3] = v[1]; };
#pragma unroll
  for (int m = 0; m < 8; ++m) {
    if (m < 5) { ldA0(m, 0); ldA1(m, 0); }
    Bf[7 - m] = *(const u32x4*)(pb + 512 * (7 - m));
    SCHED();
  }
#pragma unroll 1
  for (int dl = 0; dl < 8; ++dl) {
    pa -= 64; pa2 -= 64; pb -= 64;
#pragma unroll
    for (int m = 0; m < 8; ++m) {
#pragma unroll
      for (int J = 7; J >= m; --J) {
        acc[J] = mfma_bf(A[m], Bf[J - m], acc[J]);
        const int k = 7 - J;
        if (k == 0) Bf[7 - m] = *(const u32x4*)(pb + 512 * (7 - m));
        else if (m >= 1 && m <= 5 && k == 1) ldA0(m - 1, 0);
        else if (m >= 1 && m <= 5 && k == 2) ldA1(m - 1, 0);
        else if (m == 0 && k <= 6) { if (k & 1) ldA0(5 + (k - 1) / 2, 64); else ldA1(5 + (k - 1) / 2, 64); }
        SCHED();
      }
    }
  }
  {
    const bool wd = li & 1;
    const u32 yo = WS_YT + (d * SEQ + 32 * (li >> 1) + 8 * g + (wd ? 0 : 4)) * 2;
#pragma unroll
    for (int J = 0; J < 8; ++J) {
      const u32 p0 = pk_bf(acc[J][0], acc[J][1]), p1 = pk_bf(acc[J][2], acc[J][3]);
      const u32 rcv = __builtin_amdgcn_mov_dpp(wd ? p1 : p0, 0xB1, 0xf, 0xf, true);
      st8_wt(rs, yo + 512 * J, wd ? u32x2{p0, rcv} : u32x2{rcv, p1});
    }
  }
}

constexpr int G_WSLOT = 20480, G_XR = 102400, G_XSLOT = 16384, G_WO = 151552, G_RED = 159744, G_GW = 161792;
constexpr int G_PS = G_XR + G_XSLOT;

DI void role_gemm(const P1& P, int hg, int p, char* smem, const char* code) {
  const int tid = threadIdx.x, lane = tid & 63;
  const int w = __builtin_amdgcn_readfirstlane(tid >> 6);
  const int li = lane & 15, g = lane >> 4;
  const rsrc_t rs = ws_rsrc(P.ws);
  const int mh = p >> 2, mcq = p & 3;
  const f32x4 zero = {0.f, 0.f, 0.f, 0.f};
  const f32x4 eps4 = {1e-6f, 1e-6f, 1e-6f, 1e-6f};
  const u32x2 z2 = {0u, 0u};
  const int sw = (li >> 1) & 7;
  const char* xr = smem + G_XR + (32 * (w & 3) + li) * 128;
  const int xs0 = (g ^ sw) * 16, xs1 = ((4 + g) ^ sw) * 16;
  const int wrow = (8 * g + (li >> 2)) * 320 + (li & 3) * 8;
  const char* wr_e = smem + wrow + (g & 1) * 32;
  const char* wr_o = smem + wrow - (g & 1) * 32;
  auto wfrag = [&](int un, int ks, int t) {
    const char* a = ((t & 1) ? wr_o : wr_e) + (un % 5) * G_WSLOT + ks * 10240 + t * 32;
    return cat(tr_read(a), tr_read(a + 1280));
  };
  u32x2* red = (u32x2*)(smem + G_RED);

  if (w >= 4) {
    const int qw = w - 4;
    __builtin_amdgcn_s_setprio(3);
    const u32 lds0 = (u32)(size_t)(LDSP char*)smem;
    u32 xo[4], wc[5];
    u32 hmask = 0;
#pragma unroll
    for (int i = 0; i < 4; ++i) {
      int r = 32 * qw + 8 * i + (lane >> 3);
      int c = (lane & 7) ^ ((r >> 1) & 7);
      xo[i] = ((128 * p + r) * DM + 8 * c) * 2;
    }
    auto issue_x = [&](int j) {
      const char* sb = (const char*)P.u + j * 128;
      const u32 db = lds0 + G_XR + (j % 3) * G_XSLOT + qw * 4096;
#pragma unroll
      for (int i = 0; i < 4; ++i) glds1(sb, db + i * 1024, xo[i]);
    };
    issue_x(0);
#pragma unroll
    for (int i = 0; i < 5; ++i) {
      int id = 64 * (5 * qw + i) + lane;
      int r = id / 20, c = (id - 20 * r) ^ (((r >> 3) & 1) << 1);
      int h = c >= 10;
      wc[i] = (r * NW + 80 * hg + 8 * (c - 10 * h)) * 2;
      hmask |= h << i;
    }
    auto issue_w = [&](int un) {
      const u32 db = lds0 + (un % 5) * G_WSLOT + qw * 5120;
      const u32 dlt = un < 10 ? 1280 : 64 * NW * 2;
      const char* sb = (const char*)P.wqkv + (size_t)(un < 10 ? un : 2 * (un - 10)) * (64 * NW * 2) + (un < 10 ? 1280 : 0);
#pragma unroll
      for (int i = 0; i < 5; ++i) glds1(sb, db + i * 1024, wc[i] + (((hmask >> i) & 1) ? dlt : 0u));
    };
    glds1((const char*)P.gw, lds0 + G_GW + (qw & 1) * 1024, min((qw & 1) * 1024 + lane * 16, 1264));
    issue_w(0); issue_x(1); issue_w(1);
    const bool gate = hg == (p & 7);
    f32x4 ga[2] = {zero, zero};
    u32x4 keep[20][2];
    f32x4 aq[2][5];
#pragma unroll
    for (int c = 0; c < 5; ++c) { aq[0][c] = eps4; aq[1][c] = eps4; }
    auto lstep = [&](auto UN) {
      constexpr int un = decltype(UN)::value;
      if (un < 9) wait_vm<9>(); else wait_vm<7>();
      BAR();
#pragma unroll
      for (int r = 0; r < 2; ++r) {
        keep[2 * un][r] = *(const u32x4*)(xr + (un % 3) * G_XSLOT + r * 2048 + xs0);
        keep[2 * un + 1][r] = *(const u32x4*)(xr + (un % 3) * G_XSLOT + r * 2048 + xs1);
      }
      if (un + 2 < 10) issue_x(un + 2);
      issue_w(un + 2);
      if (gate) {
        const char* gp = smem + G_GW + un * 128 + g * 16;
        const u32x4 g0 = *(const u32x4*)gp, g1 = *(const u32x4*)(gp + 64);
#pragma unroll
        for (int r = 0; r < 2; ++r) { ga[r] = mfma_bf(keep[2 * un][r], g0, ga[r]); ga[r] = mfma_bf(keep[2 * un + 1][r], g1, ga[r]); }
      }
      if (un == 8) {
        const char* wob = (const char*)P.wo + (size_t)(80 * hg + 20 * mh) * (DM * 2) + mcq * 320;
#pragma unroll
        for (int i = 0; i < 2; ++i) {
          int id = min(64 * (2 * qw + i) + lane, 399);
          int r = id / 20, c = id - 20 * r;
          glds1(wob, lds0 + G_WO + (2 * qw + i) * 1024, r * (DM * 2) + c * 16);
        }
      }
    };
    lstep(IC<0>{}); lstep(IC<1>{}); lstep(IC<2>{}); lstep(IC<3>{}); lstep(IC<4>{});
    lstep(IC<5>{}); lstep(IC<6>{}); lstep(IC<7>{}); lstep(IC<8>{}); lstep(IC<9>{});
    {
      u32x4 bq[2][5];
      auto qgrp = [&](auto GG) {
        constexpr int G = decltype(GG)::value, N = G + 1;
        constexpr int kx = 4 * (G / 4) + 2 * (G & 1) + ((G / 2) & 1);
        if (G == 0) {
          wait_vm<0>(); BAR();
          __builtin_amdgcn_s_setprio(0);
          issue_w(12); issue_w(13); issue_w(14);
        }
        if (G == NQE) { wait_vm<0>(); BAR(); }
        if (G == 0 || G == NQE) {
#pragma unroll
          for (int n = 0; n < 5; ++n) bq[G & 1][n] = wfrag(10 + G / 4, (G / 2) & 1, 5 * (G & 1) + n);
#pragma unroll
          for (int t = 0; t < 5; ++t) asm volatile("" : "+v"(aq[0][t]), "+v"(aq[1][t]));
          asm volatile("s_nop 1" ::: "memory");
        }
        if (G == NQE) { BAR(); __builtin_amdgcn_s_setprio(1); }
        constexpr bool nxt = N < 20 && N != NQE;
#pragma unroll
        for (int t = 0; t < 5; ++t) {
          const int nt = 5 * (N & 1) + t;
          const char* na = ((nt & 1) ? wr_o : wr_e) + ((10 + N / 4) % 5) * G_WSLOT + ((N / 2) & 1) * 10240 + nt * 32;
          mfma_ip(aq[0][t], bq[G & 1][t], keep[kx][0]);
          if (nxt) { const u32x2 v = tr_read(na); bq[N & 1][t][0] = v[0]; bq[N & 1][t][1] = v[1]; }
          SCHED();
          mfma_ip(aq[1][t], bq[G & 1][t], keep[kx][1]);
          if (nxt) { const u32x2 v = tr_read(na + 1280); bq[N & 1][t][2] = v[0]; bq[N & 1][t][3] = v[1]; }
          SCHED();
        }
      };
      qgrp(IC<0>{}); qgrp(IC<1>{}); qgrp(IC<2>{}); qgrp(IC<3>{}); qgrp(IC<4>{}); qgrp(IC<5>{}); qgrp(IC<6>{});
      qgrp(IC<7>{}); qgrp(IC<8>{}); qgrp(IC<9>{}); qgrp(IC<10>{}); qgrp(IC<11>{}); qgrp(IC<12>{}); qgrp(IC<13>{});
      qgrp(IC<14>{}); qgrp(IC<15>{}); qgrp(IC<16>{}); qgrp(IC<17>{}); qgrp(IC<18>{}); qgrp(IC<19>{});
      asm volatile("s_nop 7\n\ts_nop 7" ::: "memory");
#pragma unroll
      for (int t = 0; t < 5; ++t) asm volatile("" : "+v"(aq[0][t]), "+v"(aq[1][t]));
    }
    u32x2 pq[2][5];
#pragma unroll
    for (int r = 0; r < 2; ++r)
#pragma unroll
      for (int c = 0; c < 5; ++c) pq[r][c] = pk4_bf(phi4(aq[r][c]));
#pragma unroll
    for (int r = 0; r < 2; ++r)
      asm volatile("" : "+v"(pq[r][0]), "+v"(pq[r][1]), "+v"(pq[r][2]), "+v"(pq[r][3]), "+v"(pq[r][4]));
    if (gate) {
      const f32x4 gs = (li & 1) ? ga[1] : ga[0];
      f32x4 go;
#pragma unroll
      for (int r = 0; r < 4; ++r) go[r] = __builtin_amdgcn_rcpf(1.f + __builtin_amdgcn_exp2f(-1.44269504089f * gs[r]));
      if (li < 2) st16_dev(rs, WS_GT + (128 * p + 32 * qw + 16 * li + 4 * g) * 4, __builtin_bit_cast(u32x4, go));
    }
    BAR();
    const int hs = li & 3;
    u32x2 zf[5];
#pragma unroll
    for (int c = 0; c < 5; ++c) {
      const int hc = g < c ? c - 1 : c;
      zf[c] = hs == hc ? red[128 + c * 4 + g] : z2;
    }
#pragma unroll
    for (int r = 0; r < 2; ++r) {
      f32x4 den = mfma_bf(cat(zf[0], zf[1]), cat(pq[r][0], pq[r][1]), zero);
      den = mfma_bf(cat(zf[2], zf[3]), cat(pq[r][2], pq[r][3]), den);
      den = mfma_bf(cat(zf[4], z2), cat(pq[r][4], z2), den);
      float rc[4];
#pragma unroll
      for (int j = 0; j < 4; ++j) rc[j] = __builtin_amdgcn_rcpf(den[j] + 1e-6f);
      u32x2 o[5];
#pragma unroll
      for (int c = 0; c < 5; ++c) {
        const float s = c == 0 ? rc[0] : (c == 4 ? rc[3] : (g < c ? rc[c - 1] : rc[c]));
        o[c] = u32x2{pk_bf(bf_lo(pq[r][c][0]) * s, bf_hi(pq[r][c][0]) * s), pk_bf(bf_lo(pq[r][c][1]) * s, bf_hi(pq[r][c][1]) * s)};
      }
      const u32 aqp = WS_AQ + ((8 * p + 2 * qw + r) * 20) * 1024 + lane * 16;
      st16_dev(rs, aqp + (2 * hg) * 1024, cat(o[0], o[1]));
      st16_dev(rs, aqp + (2 * hg + 1) * 1024, cat(o[2], o[3]));
      st8_wt(rs, aqp + (16 + (hg >> 1)) * 1024 + (hg & 1) * 8, o[4]);
    }
    return;
  }

  __builtin_amdgcn_s_setprio(2);
  u32 pf0, pf1;
  asm volatile("global_load_dword %0, %2, %3\n\tglobal_load_dword %1, %4, %3"
               : "=&v"(pf0), "=&v"(pf1) : "v"(tid * 128), "s"(code), "v"(min(32768 + tid * 128, CODE_BYTES - 4)) : "memory");
  const u64 tag = next_tag(P.ws + WS_EP + hg * 64);
  const Tag tg = mk_tag(tag);
  f32x4 acc[2][10];
#pragma unroll
  for (int c = 0; c < 10; ++c) { acc[0][c] = c < 5 ? eps4 : zero; acc[1][c] = acc[0][c]; }
  u32x4 b0[10], b1[10], xa[2], xb[2];
  u32 pso[11];
#pragma unroll
  for (int c = 0; c < 5; ++c) {
    const bool um = li < 4 * c;
    const int jd = g - c + (um ? 5 : 0), jo = jd + (um ? -4 : 4);
    const int eb = (16 * c + li) * 5;
    pso[2 * c] = G_PS + (w * 448 + ((jd >= 0 && jd < 5) ? eb + jd : 420 + li)) * 16;
    pso[2 * c + 1] = G_PS + (w * 448 + ((jo >= 0 && jo < 5) ? eb + jo : 420 + li)) * 16;
  }
  pso[10] = G_PS + (w * 448 + (li < 5 ? 400 + 4 * li + g : 420 + li)) * 16;
#pragma unroll
  for (int c = 0; c < 11; ++c) asm volatile("" : "+v"(pso[c]));
  auto kstep = [&](auto UN) {
    constexpr int un = decltype(UN)::value;
    BAR();
    auto rd = [&](u32x4& dst, int ks, int t, int hf) {
      const u32x2 v = tr_read(((t & 1) ? wr_o : wr_e) + (un % 5) * G_WSLOT + ks * 10240 + t * 32 + hf * 1280);
      dst[2 * hf] = v[0]; dst[2 * hf + 1] = v[1];
    };
    xa[0] = *(const u32x4*)(xr + (un % 3) * G_XSLOT + xs0);
    xa[1] = *(const u32x4*)(xr + (un % 3) * G_XSLOT + 2048 + xs0);
#pragma unroll
    for (int t = 0; t < 10; ++t) {
      if (un > 0) mfma_ip(acc[0][t], xb[0], b1[t]);
      rd(b0[t], 0, t, 0);
      SCHED();
      if (un > 0) mfma_ip(acc[1][t], xb[1], b1[t]);
      rd(b0[t], 0, t, 1);
      SCHED();
    }
#pragma unroll
    for (int t = 0; t < 10; ++t) {
      mfma_ip(acc[0][t], xa[0], b0[t]);
      if (t < 5) { rd(b1[2 * t], 1, 2 * t, 0); rd(b1[2 * t], 1, 2 * t, 1); }
      if (t == 5) xb[0] = *(const u32x4*)(xr + (un % 3) * G_XSLOT + xs1);
      SCHED();
      mfma_ip(acc[1][t], xa[1], b0[t]);
      if (t < 5) { rd(b1[2 * t + 1], 1, 2 * t + 1, 0); rd(b1[2 * t + 1], 1, 2 * t + 1, 1); }
      if (t == 5) xb[1] = *(const u32x4*)(xr + (un % 3) * G_XSLOT + 2048 + xs1);
      SCHED();
    }
  };
#pragma unroll
  for (int c = 0; c < 10; ++c) asm volatile("" : "+v"(acc[0][c]), "+v"(acc[1][c]));
  kstep(IC<0>{}); kstep(IC<1>{}); kstep(IC<2>{}); kstep(IC<3>{}); kstep(IC<4>{});
  asm volatile("s_waitcnt vmcnt(0)" : "+v"(pf0), "+v"(pf1) :: "memory");
  kstep(IC<5>{}); kstep(IC<6>{}); kstep(IC<7>{}); kstep(IC<8>{}); kstep(IC<9>{});
  BAR();
#pragma unroll
  for (int t = 0; t < 10; ++t) { acc[0][t] = mfma_bf(xb[0], b1[t], acc[0][t]); acc[1][t] = mfma_bf(xb[1], b1[t], acc[1][t]); }
  {
    u32x4 fr[10];
#pragma unroll
    for (int t = 0; t < 10; ++t)
      fr[t] = t < 5 ? cat(pk4_bf(phi4(acc[0][t])), pk4_bf(phi4(acc[1][t]))) : cat(pk4_bf(acc[0][t]), pk4_bf(acc[1][t]));
    const u32x4 ones = {0x3f803f80u, 0x3f803f80u, 0x3f803f80u, 0x3f803f80u};
    f32x4 zsel = zero;
#pragma unroll
    for (int c = 0; c < 5; ++c) {
      const f32x4 dd = mfma_bf(fr[5 + c], fr[c], zero);
      f32x4 dm = zero, dp = zero;
      if (c > 0) dm = mfma_bf(fr[4 + c], fr[c], zero);
      if (c < 4) dp = mfma_bf(fr[6 + c], fr[c], zero);
      const f32x4 dz = mfma_bf(fr[c], ones, zero);
      *(f32x4*)(smem + pso[2 * c]) = dd;
      *(f32x4*)(smem + pso[2 * c + 1]) = c == 0 ? dp : (c == 4 ? dm : (li < 4 * c ? dm : dp));
      if (c == 0 || li == c) zsel = dz;
    }
    *(f32x4*)(smem + pso[10]) = zsel;
  }
  BAR();
  {
    const f32x4* ps = (const f32x4*)(smem + G_PS);
    const int e1 = min(tid + 256, 423);
    const f32x4 v0 = (ps[tid] + ps[448 + tid]) + (ps[896 + tid] + ps[1344 + tid]);
    const f32x4 v1 = (ps[e1] + ps[448 + e1]) + (ps[896 + e1] + ps[1344 + e1]);
    const u32 o = WS_KV + (hg * 16 + p) * KV_STR;
    st16_dev(rs, o + tid * 16, tag4(v0, tg));
    if (w < 3) st16_dev(rs, o + e1 * 16, tag4(v1, tg));
  }
  BAR();
  u32x4 wf[3];
  {
    const char* lo = smem + G_WO + (4 * g + (li >> 2)) * 320 + (li & 3) * 8;
    const char* hi = smem + G_WO + (16 + (li >> 2)) * 320 + (li & 3) * 8;
#pragma unroll
    for (int k = 0; k < 3; ++k) {
      const int t = min(w + 4 * k, 9);
      wf[k] = cat(tr_read(lo + t * 32), tr_read(hi + t * 32));
    }
  }
  __builtin_amdgcn_s_setprio(0);
  {
    const int he = min(32 * w + (lane & 31), 99), ze = min(tid >> 3, 19);
    const u32 h1o = WS_KV + (hg * 16 + 8 * (lane >> 5)) * KV_STR + (mh * 100 + he) * 16;
    const u32 zo = WS_KV + (hg * 16 + 2 * (tid & 7)) * KV_STR + (400 + ze) * 16;
    const u32x4 t4 = {tg.t0, tg.t1, tg.t2, tg.t3};
    f32x4 ks, zs;
    for (u32 spin = 0; spin < SPIN_MAX; ++spin) {
      u32x4 h1[8], zv[2];
#pragma unroll
      for (int i = 0; i < 8; ++i) h1[i] = ld16_dev(rs, h1o + KV_STR * i);
      zv[0] = ld16_dev(rs, zo); zv[1] = ld16_dev(rs, zo + KV_STR);
      u32x4 bad = {0u, 0u, 0u, 0u};
      ks = zero; zs = zero;
#pragma unroll
      for (int i = 0; i < 8; ++i) { const u32x4 dd = h1[i] ^ t4; bad |= dd; ks += __builtin_bit_cast(f32x4, dd); }
#pragma unroll
      for (int i = 0; i < 2; ++i) { const u32x4 dd = zv[i] ^ t4; bad |= dd; zs += __builtin_bit_cast(f32x4, dd); }
      if (__all((((bad[0] | bad[1]) | (bad[2] | bad[3])) & 0xfff) == 0)) break;
      asm volatile("" ::: "memory");
    }
    __builtin_amdgcn_s_setprio(2);
#pragma unroll
    for (int r = 0; r < 4; ++r) {
      const float kr = ks[r];
      const auto sw2 = __builtin_amdgcn_permlane32_swap(__float_as_uint(kr), __float_as_uint(kr), false, false);
      ks[r] = __uint_as_float(sw2[0]) + __uint_as_float(sw2[1]);
      zs[r] = dpp_add<0x141>(dpp_add<0x4E>(dpp_add<0xB1>(zs[r])));
    }
    if (lane < 32) red[32 * w + lane] = pk4_bf(ks);
    if ((tid & 7) == 0 && tid < 160) red[128 + ze] = pk4_bf(zs);
  }
  BAR();
  if (p == 15 && tid == 0) st8_dev(P.ws + WS_EP + hg * 64, tag);
  {
    const int ib = 16 + (li & 3);
    const u32x4 a0 = cat(red[li * 5 + g], g == 0 ? red[li * 5 + 4] : z2);
    const u32x4 a1 = cat(red[ib * 5 + g], g == 0 ? red[ib * 5 + 4] : z2);
    auto dst = [&](int qi) {
      const int mc = qi >> 2;
      const int ksl = mc < 4 ? 2 * hg + (mc >> 1) : 16 + (hg >> 1), hf = mc < 4 ? (mc & 1) : (hg & 1);
      return (u32)(ksl * 1024 + (li + 16 * (qi & 3)) * 16 + hf * 8);
    };
    const u32 cb = WS_MW + (10 * mcq + w) * 20480;
    const u32 d0 = dst(5 * mh + g), d1 = dst(5 * mh + 4);
    u32x2 mb = z2;
#pragma unroll
    for (int k = 0; k < 3; ++k) {
      const u32x2 m0 = pk4_bf(mfma_bf(a0, wf[k], zero)), m1 = pk4_bf(mfma_bf(a1, wf[k], zero));
      if (k < 2 || w < 2) st8_wt(rs, cb + k * (4 * 20480) + d0, m0);
      if (g == k) mb = m1;
    }
    if (g < 2 || (g == 2 && w < 2)) st8_wt(rs, cb + g * (4 * 20480) + d1, mb);
  }
}

__global__ __launch_bounds__(512, 2) void k1(P1 P) {
  extern __shared__ __attribute__((aligned(16))) char smem[];
  const char* code;
  asm volatile("s_getpc_b64 %0" : "=s"(code));
  const int b = blockIdx.x, x = b & 7, r = b >> 3;
  if (r < 16) {
    const int hl = r & 3, pl = r >> 2;
    role_gemm(P, (x & 1) * 4 + hl, (x >> 1) * 4 + pl, smem, code);
  } else if (r < 26) {
    role_conv(P, x * 10 + (r - 16), smem);
  } else {
    const int j = (r - 26) * 512 + threadIdx.x;
    u32 s = 0;
#pragma unroll
    for (int i = 0; i < 4; ++i) s ^= *(const u32*)pf_addr(P, x, i ? 2560 + i * 3072 + j : j);
    asm volatile("" :: "v"(s));
  }
}

constexpr int K2_YA = 102400, K2_OUT = K2_YA + 12288, K2_LDS = K2_OUT + 4 * 2816;
__global__ __launch_bounds__(512, 2) void k2(const char* ws, u16* out) {
  extern __shared__ __attribute__((aligned(16))) char smem[];
  const int tid = threadIdx.x, lane = tid & 63, li = lane & 15, g = lane >> 4;
  const int wv = __builtin_amdgcn_readfirstlane(tid >> 6), w = wv & 3;
  const int b = blockIdx.x, x = b & 7, r_ = b >> 3;
  const int rg = (x >> 1) * 8 + (r_ >> 2), cg = (x & 1) * 4 + (r_ & 3);
  const int rt = rg * 4 + w;
  const u32 lo16 = lane * 16;
  if (wv >= 4) {
    const u32 lds0 = (u32)(size_t)(LDSP char*)smem;
    const char* mb = ws + WS_MW + (size_t)cg * 5 * 20480;
#pragma unroll
    for (int q = 0; q < 5; ++q) {
      const int ks = 4 * q + w;
#pragma unroll
      for (int i = 0; i < 5; ++i) glds1(mb + ks * 1024 + i * 20480, lds0 + (4 * q + w) * 5120 + i * 1024, lo16);
      if (q == 1) {
#pragma unroll
        for (int i = 0; i < 3; ++i) {
          const int ch = min(8 * (3 * w + i) + (lane >> 3), 79);
          glds1(ws + WS_YT + rg * 128, lds0 + K2_YA + (3 * w + i) * 1024, (cg * 80 + ch) * (SEQ * 2) + (((lane & 7) ^ (ch & 6)) * 16));
        }
      }
      if (q == 2) wait_vm_bar<13>(); else if (q > 2) wait_vm_bar<10>();
    }
    wait_vm_bar<5>();
    wait_vm_bar<0>();
    return;
  }
  const char* ab = ws + WS_AQ + (size_t)rt * 20480 + lo16;
  u32x4 a[20];
#pragma unroll
  for (int s = 0; s < 4; ++s) a[s] = *(const u32x4*)(ab + s * 1024);
  const float gt = *(const float*)(ws + WS_GT + rt * 64 + li * 4);
  f32x4 gy[5];
  f32x4 acc[5];
#pragma unroll
  for (int c = 0; c < 5; ++c) acc[c] = f32x4{0.f, 0.f, 0.f, 0.f};
  asm volatile("" : "+v"(acc[0]), "+v"(acc[1]), "+v"(acc[2]), "+v"(acc[3]), "+v"(acc[4]));
  const char* mrd = smem + lane * 16;
  u32x4 bf[20];
#pragma unroll
  for (int q = 0; q < 6; ++q) {
    if (q < 5) asm volatile("s_barrier" ::: "memory");
#pragma unroll
    for (int i = 0; i < 20; ++i) {
      if (q > 0) mfma_ip(acc[i % 5], bf[i], a[4 * (q - 1) + i / 5]);
      if (q < 5) bf[i] = *(const u32x4*)(mrd + (q * 20 + i) * 1024);
      if (q < 4 && i % 5 == 4) a[4 + 4 * q + i / 5] = *(const u32x4*)(ab + (4 + 4 * q + i / 5) * 1024);
      SCHED();
    }
    if (q == 2) {
      const int chl = 4 * g + (li >> 2);
      const char* yr = smem + K2_YA + chl * 128 + (((2 * w) ^ (chl & 6)) * 16) + (li & 3) * 8;
#pragma unroll
      for (int c = 0; c < 5; ++c) {
        const u32x2 ya = tr_read(yr + c * 2048);
        gy[c] = f32x4{bf_lo(ya[0]), bf_hi(ya[0]), bf_lo(ya[1]), bf_hi(ya[1])} * gt;
      }
      asm volatile("" : "+v"(gy[0]), "+v"(gy[1]), "+v"(gy[2]), "+v"(gy[3]), "+v"(gy[4]));
    }
  }
  asm volatile("s_nop 7\n\ts_nop 7" : "+v"(acc[0]), "+v"(acc[1]), "+v"(acc[2]), "+v"(acc[3]), "+v"(acc[4]));
  char* ot = smem + K2_OUT + w * 2816;
  const float og = 1.f - gt;
#pragma unroll
  for (int c = 0; c < 5; ++c) *(u32x2*)(ot + li * 176 + c * 32 + g * 8) = pk4_bf(acc[c] * og + gy[c]);
  MEMB();
#pragma unroll
  for (int i = 0; i < 3; ++i) {
    const int id = 64 * i + lane, r = id / 10, c = id - 10 * r;
    if (id < 160) *(u32x4*)(out + (size_t)(rt * 16 + r) * DM + cg * 80 + c * 8) = *(const u32x4*)(ot + r * 176 + c * 16);
  }
}

extern "C" void kernel_launch(void* const* d_in, const int* in_sizes, int n_in, void* d_out, int out_size,
                              void* d_ws, size_t ws_size, hipStream_t stream) {
  P1 P;
  P.u = (const u16*)d_in[0]; P.are = (const u16*)d_in[1]; P.aim = (const u16*)d_in[2];
  P.bre = (const u16*)d_in[3]; P.bim = (const u16*)d_in[4]; P.cre = (const u16*)d_in[5]; P.cim = (const u16*)d_in[6];
  P.logdt = (const u16*)d_in[7]; P.dskip = (const u16*)d_in[8]; P.wqkv = (const u16*)d_in[10];
  P.wo = (const u16*)d_in[11]; P.gw = (const u16*)d_in[12]; P.ws = (char*)d_ws;
  (void)hipFuncSetAttribute((const void*)k1, hipFuncAttributeMaxDynamicSharedMemorySize, 163840);
  (void)hipFuncSetAttribute((const void*)k2, hipFuncAttributeMaxDynamicSharedMemorySize, K2_LDS);
  k1<<<256, 512, 163840, stream>>>(P);
  k2<<<256, 512, K2_LDS, stream>>>((const char*)d_ws, (u16*)d_out);
}
```
